```python
import torch

def _softmax(x, axis=-1):
    return torch.softmax(x, dim=axis)
def _gelu(x):
    return torch.nn.functional.gelu(x, approximate="tanh")
def _silu(x):
    return torch.nn.functional.silu(x)
def _sigmoid(x):
    return torch.sigmoid(x)
def _layernorm(x, axis=-1, eps=1e-5):
    m = x.mean(dim=axis, keepdim=True)
    v = x.var(dim=axis, keepdim=True, unbiased=False)
    return (x - m) / torch.sqrt(v + eps)
def _scatter_add(tgt, idx, src, axis=0):
    return tgt.clone().index_add_(axis, idx, src)
def _segment_sum(src, seg, n):
    out = torch.zeros((n,) + src.shape[1:], dtype=src.dtype)
    return out.index_add_(0, seg, src)
def _pad(x, pw):
    flat = tuple(v for pair in reversed(pw) for v in pair)
    return torch.nn.functional.pad(x, flat)

kernel_name = 'parallel_sum__sliding_block_cached__glu_gated_glu__decode'

def setup_inputs():

    _w = (lambda a: (lambda t: t.to(torch.bfloat16) if t.is_floating_point() else t)(torch.as_tensor(a)))
    def _A_setup():
        B, Sq, D = 1, 1, 640
        Hq, Hkv, Sk, Dh = 32, 32, 32768, 128
        assert Hq % Hkv == 0, (Hq, Hkv)
        return {
            "x": _w((lambda *s: torch.randn(*s, dtype=torch.bfloat16) * 0.05)(B, Sq, D)),
            "k_cache": _w((lambda *s: torch.randn(*s, dtype=torch.bfloat16) * 3.2)(B, Hkv, Sk, Dh)),
            "v_cache": _w((lambda *s: torch.randn(*s, dtype=torch.bfloat16) * 1.6)(B, Hkv, Sk, Dh)),
            "wq": _w((lambda *s: torch.randn(*s, dtype=torch.bfloat16) * 3.2)(D, Hq * Dh) * (D ** -0.5)),
            "wo": _w((lambda *s: torch.randn(*s, dtype=torch.bfloat16) * 1.6)(Hq * Dh, D) * ((Hq * Dh) ** -0.5)),
        }
    _A_in = _A_setup()

    def _B_setup():
        B, S, D, F = 1, 1, 640, 1728
        return {
            "x": _w((lambda *s: torch.randn(*s, dtype=torch.bfloat16) * 0.05)(B, S, D)),
            "w_gate": _w((lambda *s: torch.randn(*s, dtype=torch.bfloat16) * 0.2)(D, F)),
            "w_up": _w((lambda *s: torch.randn(*s, dtype=torch.bfloat16) * 0.05)(D, F)),
            "w_down": _w((lambda *s: torch.randn(*s, dtype=torch.bfloat16) * 0.05)(F, D)),
        }
    _B_in = _B_setup()

    return {
        "A_x": _A_in["x"],
        "A_k_cache": _A_in["k_cache"],
        "A_v_cache": _A_in["v_cache"],
        "A_wq": _A_in["wq"],
        "A_wo": _A_in["wo"],
        "B_x": _B_in["x"],
        "B_w_gate": _B_in["w_gate"],
        "B_w_up": _B_in["w_up"],
        "B_w_down": _B_in["w_down"],
    }

def reference(A_x, A_k_cache, A_v_cache, A_wq, A_wo, B_x, B_w_gate, B_w_up, B_w_down):
    def _A_ref(x, k_cache, v_cache, wq, wo):
        B, Sq, D = x.shape
        Hq, Dh = 32, 128
        Hkv, Sk = k_cache.shape[1], k_cache.shape[2]
        W = min(4096, max(256, Sk // 4))
        rep = Hq // Hkv
        q = (lambda x, *d: x.permute(*d))((x @ wq).reshape(B, Sq, Hq, Dh), 0, 2, 1, 3)
        k = (lambda x, n, axis: torch.repeat_interleave(x, n, dim=axis))(k_cache[:, :, Sk - W:, :], rep, axis=1)
        v = (lambda x, n, axis: torch.repeat_interleave(x, n, dim=axis))(v_cache[:, :, Sk - W:, :], rep, axis=1)
        scores = torch.einsum("bhqd,bhkd->bhqk", q, k) * (Dh ** -0.5)
        p = _softmax(scores, axis=-1)
        o = torch.einsum("bhqk,bhkd->bhqd", p, v)
        return (lambda x, *d: x.permute(*d))(o, 0, 2, 1, 3).reshape(B, Sq, Hq * Dh) @ wo

    def _B_ref(x, w_gate, w_up, w_down):
        g = torch.einsum("bsd,df->bsf", x, w_gate)
        u = torch.einsum("bsd,df->bsf", x, w_up)
        h = _sigmoid(g) * u
        return torch.einsum("bsf,fd->bsd", h, w_down)

    x = A_x
    xn = _layernorm(x, axis=-1)
    return x + _A_ref(xn, A_k_cache, A_v_cache, A_wq, A_wo) + _B_ref(xn, B_w_gate, B_w_up, B_w_down)
```

```cpp
#include <hip/hip_runtime.h>
#include <hip/hip_bf16.h>
#include <stdint.h>

#define GAS __attribute__((address_space(1)))
typedef unsigned v4u __attribute__((ext_vector_type(4)));
typedef unsigned v2u __attribute__((ext_vector_type(2)));
typedef float f32x2 __attribute__((ext_vector_type(2)));
typedef float f32x4 __attribute__((ext_vector_type(4)));
typedef __attribute__((ext_vector_type(2))) __bf16 bf16x2;
typedef GAS unsigned gu32;
typedef GAS unsigned long long gu64;
typedef GAS const unsigned short gcu16;
typedef GAS const unsigned gcu32;
typedef GAS const v4u gcv4u;
#define RLX_AGENT __ATOMIC_RELAXED, __HIP_MEMORY_SCOPE_AGENT

constexpr int D = 640, HQ = 32, DH = 128, SK = 32768, WIN = 4096, F = 1728;
constexpr int NSPLIT = 8, KPW = WIN / NSPLIT;
constexpr int NT = 1024, NWAVE = 16;
constexpr int NFG = F / 64;
constexpr unsigned SPIN_CAP = 1u << 16;

constexpr int WS_EPOCH = 0;
constexpr int WS_OG = 64;
constexpr int WS_SG = WS_OG + HQ * NSPLIT * DH;
constexpr int WS_FG = WS_SG + HQ * 16;
constexpr int WS_PG = WS_FG + NFG * NSPLIT * 128;

__device__ __forceinline__ float bf_lo(unsigned w) { return __builtin_bit_cast(float, w << 16); }
__device__ __forceinline__ float bf_hi(unsigned w) { return __builtin_bit_cast(float, w & 0xffff0000u); }
__device__ __forceinline__ unsigned cvtpk(float lo, float hi) {
  unsigned r;
  asm("v_cvt_pk_bf16_f32 %0, %1, %2" : "=v"(r) : "v"(lo), "v"(hi));
  return r;
}
__device__ __forceinline__ float rbf(float x) { return bf_lo(cvtpk(x, x)); }
__device__ __forceinline__ float dot2(unsigned a, unsigned b, float acc) {
  return __builtin_amdgcn_fdot2_f32_bf16(__builtin_bit_cast(bf16x2, a), __builtin_bit_cast(bf16x2, b), acc, false);
}
template <int CTRL> __device__ __forceinline__ float dpp(float x) {
  return __builtin_bit_cast(float, __builtin_amdgcn_mov_dpp(__builtin_bit_cast(int, x), CTRL, 0xf, 0xf, true));
}
constexpr int XOR1 = 0xB1, XOR2 = 0x4E, HMIR = 0x141, ROR8 = 0x128;
__device__ __forceinline__ float row_sum(float x) {
  x += dpp<XOR1>(x); x += dpp<XOR2>(x); x += dpp<HMIR>(x); x += dpp<ROR8>(x);
  return x;
}
__device__ __forceinline__ float fold32(float a, float b) {
  auto t = __builtin_amdgcn_permlane32_swap(__float_as_uint(a), __float_as_uint(b), false, false);
  return __uint_as_float(t[0]) + __uint_as_float(t[1]);
}
__device__ __forceinline__ float fold16(float a, float b) {
  auto t = __builtin_amdgcn_permlane16_swap(__float_as_uint(a), __float_as_uint(b), false, false);
  return __uint_as_float(t[0]) + __uint_as_float(t[1]);
}
__device__ __forceinline__ float xrow_sum(float x) { x = fold16(x, x); return fold32(x, x); }
__device__ __forceinline__ float xrow_max(float x) {
  auto s = __builtin_amdgcn_permlane16_swap(__float_as_uint(x), __float_as_uint(x), false, false);
  x = fmaxf(__uint_as_float(s[0]), __uint_as_float(s[1]));
  auto t = __builtin_amdgcn_permlane32_swap(__float_as_uint(x), __float_as_uint(x), false, false);
  return fmaxf(__uint_as_float(t[0]), __uint_as_float(t[1]));
}
__device__ __forceinline__ float wave_sum(float x) { return xrow_sum(row_sum(x)); }
__device__ __forceinline__ f32x2 fold8(const float (&v)[8]) {
  float t0 = fold32(v[0], v[4]), t1 = fold32(v[1], v[5]), t2 = fold32(v[2], v[6]), t3 = fold32(v[3], v[7]);
  f32x2 u;
  u.x = fold16(t0, t2);
  u.y = fold16(t1, t3);
  return u;
}

__device__ __forceinline__ void put(gu64* g, float v, unsigned tag32, unsigned tag8) {
  const unsigned lo = (__float_as_uint(v) & 0xffffff00u) | tag8;
  __hip_atomic_store(g, ((unsigned long long)tag32 << 32) | lo, RLX_AGENT);
}
template <int N>
__device__ __forceinline__ void gather(gu64* g, const int (&idx)[N], unsigned tag32, unsigned tag8, float (&v)[N]) {
  for (unsigned spins = 0;; ++spins) {
    bool ok = true;
    unsigned long long x[N];
#pragma unroll
    for (int k = 0; k < N; ++k) x[k] = __hip_atomic_load(g + idx[k], RLX_AGENT);
#pragma unroll
    for (int k = 0; k < N; ++k) {
      const unsigned lo = (unsigned)x[k], hi = (unsigned)(x[k] >> 32);
      ok &= (hi == tag32) & ((lo & 0xffu) == tag8);
      v[k] = __uint_as_float(lo & 0xffffff00u);
    }
    if (__all(ok)) return;
    if (spins > SPIN_CAP) {
#pragma unroll
      for (int k = 0; k < N; ++k) v[k] = __uint_as_float(0x7fc00000u);
      return;
    }
    __builtin_amdgcn_s_sleep(1);
  }
}

__device__ __forceinline__ v4u ld_nt(gcv4u* p) { return __builtin_nontemporal_load(p); }

__global__ void __launch_bounds__(NT, 4)
fused_decode(const unsigned short* __restrict__ x_, const unsigned short* __restrict__ kc_, const unsigned short* __restrict__ vc_,
             const unsigned short* __restrict__ wq_, const unsigned short* __restrict__ wo_, const unsigned short* __restrict__ wg_,
             const unsigned short* __restrict__ wu_, const unsigned short* __restrict__ wd_, unsigned short* __restrict__ out_,
             unsigned long long* ws_) {
  __shared__ __attribute__((aligned(16))) float s_q[NWAVE * 128];
  __shared__ __attribute__((aligned(16))) float s_gu[10 * 128];
  __shared__ __attribute__((aligned(16))) float s_acc[NWAVE * 128];
  __shared__ __attribute__((aligned(16))) float s_ml[NWAVE * 2];
  __shared__ __attribute__((aligned(16))) float s_o[128];
  __shared__ __attribute__((aligned(16))) float s_h[64];
  __shared__ __attribute__((aligned(16))) float s_p[NWAVE * 128];

  const int b = blockIdx.x, xcd = b & 7, bi = b >> 3;
  const int h = (bi >> 3) * 8 + xcd, s = bi & 7;
  const int tid = threadIdx.x, lane = tid & 63, w = __builtin_amdgcn_readfirstlane(tid >> 6), r = lane >> 4, c = lane & 15;
  const bool ffn = h < NFG;
  gu64* ws = (gu64*)ws_;

  gcu16* x = (gcu16*)x_;
  const unsigned long long ep = __hip_atomic_load(ws + WS_EPOCH, RLX_AGENT);
  unsigned xa[5], xb[5];
  {
    gcu32* pa = (gcu32*)(x + 10 * lane);
    gcu32* pb = (gcu32*)(x + 10 * (4 * w + r));
#pragma unroll
    for (int i = 0; i < 5; ++i) { xa[i] = pa[i]; xb[i] = pb[i]; }
  }
  const int grow = 80 * s + 8 * (w < 10 ? w : 0) + (lane >> 3);
  const unsigned xg = x[grow];
  v4u wqv[10], kv[8], vv[8], gv, uv;
  gcu16* kb = (gcu16*)kc_ + ((size_t)h * SK + (SK - WIN) + s * KPW + 32 * w + r) * DH + 8 * c;
  gcu16* vb = (gcu16*)vc_ + ((size_t)h * SK + (SK - WIN) + s * KPW + 32 * w + r) * DH + 8 * c;
  gcu16* qb = (gcu16*)wq_ + (size_t)(40 * w + 10 * r) * (HQ * DH) + h * DH + 8 * c;
#pragma unroll
  for (int i = 0; i < 10; ++i) {
    wqv[i] = *(gcv4u*)(qb + (size_t)i * (HQ * DH));
    if (i < 8) kv[i] = ld_nt((gcv4u*)(kb + i * 4 * DH));
  }
  if (ffn && w < 10) {
    gv = ld_nt((gcv4u*)((gcu16*)wg_ + (size_t)grow * F + 64 * h + 8 * (lane & 7)));
    uv = ld_nt((gcv4u*)((gcu16*)wu_ + (size_t)grow * F + 64 * h + 8 * (lane & 7)));
  } else {
    gv = v4u{0, 0, 0, 0}; uv = v4u{0, 0, 0, 0};
  }
  __builtin_amdgcn_sched_barrier(0);

  const unsigned e_lo = __builtin_amdgcn_readfirstlane((unsigned)ep), e_hi = __builtin_amdgcn_readfirstlane((unsigned)(ep >> 32));
  unsigned tag32 = (e_lo + 1u) * 0x9E3779B1u; tag32 ^= tag32 >> 15; tag32 *= 0x85EBCA77u; tag32 ^= tag32 >> 13; tag32 ^= 0x5bd1e995u;
  const unsigned tag8 = (((e_hi ^ (e_lo * 0xC2B2AE35u)) * 0x27D4EB2Fu) >> 24) ^ 0x5Au;

  float mean_b, den;
  {
    float xs[10];
#pragma unroll
    for (int i = 0; i < 5; ++i) { xs[2 * i] = bf_lo(xa[i]); xs[2 * i + 1] = bf_hi(xa[i]); }
    float sum = 0.f;
#pragma unroll
    for (int i = 0; i < 10; ++i) sum += xs[i];
    const float mean = wave_sum(sum) * (1.f / D);
    float sq = 0.f;
#pragma unroll
    for (int i = 0; i < 10; ++i) { const float d = xs[i] - mean; sq = fmaf(d, d, sq); }
    const float var = wave_sum(sq) * (1.f / D);
    mean_b = rbf(mean);
    den = rbf(sqrtf(rbf(rbf(var) + 1e-5f)));
  }
  {
    float acc[8];
#pragma unroll
    for (int k = 0; k < 8; ++k) acc[k] = 0.f;
#pragma unroll
    for (int i = 0; i < 10; ++i) {
      const float xv = (i & 1) ? bf_hi(xb[i >> 1]) : bf_lo(xb[i >> 1]);
      const unsigned xn_hi = __float_as_uint(rbf(rbf(xv - mean_b) / den));
      const unsigned xn_lo = xn_hi >> 16;
#pragma unroll
      for (int k = 0; k < 4; ++k) {
        acc[2 * k] = dot2(wqv[i][k], xn_lo, acc[2 * k]);
        acc[2 * k + 1] = dot2(wqv[i][k], xn_hi, acc[2 * k + 1]);
      }
    }
    const f32x2 u = fold8(acc);
    *(f32x2*)(s_q + w * 128 + 8 * c + 2 * r) = u;
  }
#pragma unroll
  for (int i = 0; i < 8; ++i) vv[i] = ld_nt((gcv4u*)(vb + i * 4 * DH));
  const int rho = 4 * w + r, cc = c < 10 ? c : 9;
  v4u woa = *(gcv4u*)((gcu16*)wo_ + (size_t)(h * DH + rho) * D + 80 * s + 8 * cc);
  v4u wob = *(gcv4u*)((gcu16*)wo_ + (size_t)(h * DH + rho + 64) * D + 80 * s + 8 * cc);
  v4u wdv = *(gcv4u*)((gcu16*)wd_ + (size_t)((ffn ? h : 0) * 64 + rho) * D + 80 * s + 8 * cc);
  __builtin_amdgcn_sched_barrier(0);
  if (ffn && w < 10) {
    const float xn = rbf(rbf(bf_lo(xg) - mean_b) / den);
    float g[8], u[8];
#pragma unroll
    for (int k = 0; k < 4; ++k) {
      g[2 * k] = xn * bf_lo(gv[k]); g[2 * k + 1] = xn * bf_hi(gv[k]);
      u[2 * k] = xn * bf_lo(uv[k]); u[2 * k + 1] = xn * bf_hi(uv[k]);
    }
    f32x2 gs = fold8(g), us = fold8(u);
    gs.x += dpp<ROR8>(gs.x); gs.y += dpp<ROR8>(gs.y);
    us.x += dpp<ROR8>(us.x); us.y += dpp<ROR8>(us.y);
    *(f32x2*)(s_gu + w * 128 + (c < 8 ? 0 : 64) + 8 * (c & 7) + 2 * r) = (c < 8) ? gs : us;
  }
  __syncthreads();
  unsigned qp[4];
  {
    float q8[8];
#pragma unroll
    for (int k = 0; k < 8; ++k) q8[k] = 0.f;
#pragma unroll
    for (int t = 0; t < 4; ++t) {
      const f32x4 a = *(const f32x4*)(s_q + (4 * r + t) * 128 + 8 * c), bq = *(const f32x4*)(s_q + (4 * r + t) * 128 + 8 * c + 4);
      q8[0] += a.x; q8[1] += a.y; q8[2] += a.z; q8[3] += a.w; q8[4] += bq.x; q8[5] += bq.y; q8[6] += bq.z; q8[7] += bq.w;
    }
#pragma unroll
    for (int k = 0; k < 8; ++k) q8[k] = xrow_sum(q8[k]);
#pragma unroll
    for (int k = 0; k < 4; ++k) qp[k] = cvtpk(q8[2 * k], q8[2 * k + 1]);
  }
  if (ffn && w == 15) {
    float g = 0.f, u = 0.f;
#pragma unroll
    for (int t = 0; t < 10; ++t) { g += s_gu[t * 128 + lane]; u += s_gu[t * 128 + 64 + lane]; }
    gu64* fg = ws + WS_FG + (h * NSPLIT + s) * 128;
    put(fg + lane, g, tag32, tag8);
    put(fg + 64 + lane, u, tag32, tag8);
  }
  float p[8], m_w, l_w;
  {
    constexpr float SCALE = 0.08838834764831845f, LOG2E = 1.4426950408889634f;
    float sc[8];
#pragma unroll
    for (int i = 0; i < 8; ++i) {
      float d = 0.f;
#pragma unroll
      for (int k = 0; k < 4; ++k) d = dot2(kv[i][k], qp[k], d);
      sc[i] = rbf(rbf(row_sum(d)) * SCALE);
    }
    float m = sc[0];
#pragma unroll
    for (int i = 1; i < 8; ++i) m = fmaxf(m, sc[i]);
    m_w = xrow_max(m);
    float l = 0.f;
#pragma unroll
    for (int i = 0; i < 8; ++i) { p[i] = __builtin_amdgcn_exp2f((sc[i] - m_w) * LOG2E); l += p[i]; }
    l_w = xrow_sum(l);
  }
  {
    float acc[8];
#pragma unroll
    for (int k = 0; k < 8; ++k) acc[k] = 0.f;
#pragma unroll
    for (int i = 0; i < 8; ++i)
#pragma unroll
      for (int k = 0; k < 4; ++k) {
        acc[2 * k] = fmaf(p[i], bf_lo(vv[i][k]), acc[2 * k]);
        acc[2 * k + 1] = fmaf(p[i], bf_hi(vv[i][k]), acc[2 * k + 1]);
      }
    const f32x2 u = fold8(acc);
    *(f32x2*)(s_acc + w * 128 + 8 * c + 2 * r) = u;
    if (lane == 0) *(f32x2*)(s_ml + 2 * w) = f32x2{m_w, l_w};
  }
  __syncthreads();
  constexpr float LOG2E = 1.4426950408889634f;
  if (w == 0) {
    f32x2 ml[NWAVE], a[NWAVE];
#pragma unroll
    for (int t = 0; t < NWAVE; ++t) { ml[t] = *(const f32x2*)(s_ml + 2 * t); a[t] = *(const f32x2*)(s_acc + t * 128 + 2 * lane); }
    float M = ml[0].x;
#pragma unroll
    for (int t = 1; t < NWAVE; ++t) M = fmaxf(M, ml[t].x);
    float L = 0.f, o0 = 0.f, o1 = 0.f;
#pragma unroll
    for (int t = 0; t < NWAVE; ++t) {
      const float f = __builtin_amdgcn_exp2f((ml[t].x - M) * LOG2E);
      L = fmaf(f, ml[t].y, L); o0 = fmaf(f, a[t].x, o0); o1 = fmaf(f, a[t].y, o1);
    }
    gu64* og = ws + WS_OG + (h * NSPLIT) * DH;
    gu64* sg = ws + WS_SG + h * 16;
    put(og + s * DH + lane, o0, tag32, tag8);
    put(og + s * DH + 64 + lane, o1, tag32, tag8);
    if (lane < 2) put(sg + 2 * s + lane, lane == 0 ? M : L, tag32, tag8);
    int idx[17];
#pragma unroll
    for (int t = 0; t < 8; ++t) { idx[2 * t] = WS_OG + (h * NSPLIT + t) * DH + lane; idx[2 * t + 1] = idx[2 * t] + 64; }
    idx[16] = WS_SG + h * 16 + (lane & 15);
    float v[17];
    gather<17>(ws, idx, tag32, tag8, v);
    float ms[8], ls[8];
#pragma unroll
    for (int t = 0; t < 8; ++t) {
      ms[t] = __builtin_bit_cast(float, __builtin_amdgcn_readlane(__builtin_bit_cast(int, v[16]), 2 * t));
      ls[t] = __builtin_bit_cast(float, __builtin_amdgcn_readlane(__builtin_bit_cast(int, v[16]), 2 * t + 1));
    }
    float MM = ms[0];
#pragma unroll
    for (int t = 1; t < 8; ++t) MM = fmaxf(MM, ms[t]);
    float LL = 0.f, a0 = 0.f, a1 = 0.f;
#pragma unroll
    for (int t = 0; t < 8; ++t) {
      const float f = __builtin_amdgcn_exp2f((ms[t] - MM) * LOG2E);
      LL = fmaf(f, ls[t], LL); a0 = fmaf(f, v[2 * t], a0); a1 = fmaf(f, v[2 * t + 1], a1);
    }
    const float inv = 1.f / LL;
    *(f32x2*)(s_o + 2 * lane) = f32x2{rbf(a0 * inv), rbf(a1 * inv)};
  } else if (w == 1) {
    float hv = 0.f;
    if (ffn) {
      int idx[16];
#pragma unroll
      for (int t = 0; t < 8; ++t) { idx[2 * t] = WS_FG + (h * NSPLIT + t) * 128 + lane; idx[2 * t + 1] = idx[2 * t] + 64; }
      float v[16];
      gather<16>(ws, idx, tag32, tag8, v);
      float g = 0.f, u = 0.f;
#pragma unroll
      for (int t = 0; t < 8; ++t) { g += v[2 * t]; u += v[2 * t + 1]; }
      g = rbf(g); u = rbf(u);
      const float sg = rbf(1.f / (1.f + __builtin_amdgcn_exp2f(-g * LOG2E)));
      hv = rbf(sg * u);
    }
    s_h[lane] = hv;
  }
  __syncthreads();
  {
    const float oa = s_o[rho], ob = s_o[rho + 64], hh = s_h[rho];
    float acc[8];
#pragma unroll
    for (int k = 0; k < 4; ++k) {
      acc[2 * k] = fmaf(oa, bf_lo(woa[k]), fmaf(ob, bf_lo(wob[k]), hh * bf_lo(wdv[k])));
      acc[2 * k + 1] = fmaf(oa, bf_hi(woa[k]), fmaf(ob, bf_hi(wob[k]), hh * bf_hi(wdv[k])));
    }
    const f32x2 u = fold8(acc);
    *(f32x2*)(s_p + w * 128 + 8 * c + 2 * r) = u;
  }
  __syncthreads();
  if (tid < 80) {
    float a = 0.f;
#pragma unroll
    for (int t = 0; t < NWAVE; ++t) a += s_p[t * 128 + tid];
    put(ws + WS_PG + ((s * 5 + (tid >> 4)) * 32 + h) * 16 + (tid & 15), a, tag32, tag8);
  } else if (w == 2 && h < 5) {
    int idx[8];
#pragma unroll
    for (int t = 0; t < 8; ++t) idx[t] = WS_PG + ((s * 5 + h) * 32 + 8 * r + t) * 16 + c;
    const int col = 80 * s + 16 * h + c;
    const float xv = bf_lo(x[col]);
    float v[8];
    gather<8>(ws, idx, tag32, tag8, v);
    float a = 0.f;
#pragma unroll
    for (int t = 0; t < 8; ++t) a += v[t];
    a = xrow_sum(a);
    if (lane < 16) ((GAS unsigned short*)out_)[col] = (unsigned short)(cvtpk(xv + a, 0.f) & 0xffffu);
    if (b == 0 && lane == 0) __hip_atomic_store(ws + WS_EPOCH, ep + 1ull, RLX_AGENT);
  }
}

extern "C" void kernel_launch(void* const* d_in, const int* in_sizes, int n_in, void* d_out, int out_size, void* d_ws,
                              size_t ws_size, hipStream_t stream) {
  (void)in_sizes; (void)n_in; (void)out_size; (void)ws_size;
  hipLaunchKernelGGL(fused_decode, dim3(256), dim3(NT), 0, stream, (const unsigned short*)d_in[0], (const unsigned short*)d_in[1],
                     (const unsigned short*)d_in[2], (const unsigned short*)d_in[3], (const unsigned short*)d_in[4],
                     (const unsigned short*)d_in[6], (const unsigned short*)d_in[7], (const unsigned short*)d_in[8],
                     (unsigned short*)d_out, (unsigned long long*)d_ws);
}
```

```cpp
#include <hip/hip_runtime.h>
#include <hip/hip_bf16.h>
#include <stdint.h>

#define GAS __attribute__((address_space(1)))
typedef unsigned v4u __attribute__((ext_vector_type(4)));
typedef unsigned v2u __attribute__((ext_vector_type(2)));
typedef float f32x2 __attribute__((ext_vector_type(2)));
typedef float f32x4 __attribute__((ext_vector_type(4)));
typedef __attribute__((ext_vector_type(2))) __bf16 bf16x2;
typedef GAS unsigned gu32;
typedef GAS unsigned long long gu64;
typedef GAS const unsigned short gcu16;
typedef GAS const unsigned gcu32;
typedef GAS const v4u gcv4u;
#define RLX_AGENT __ATOMIC_RELAXED, __HIP_MEMORY_SCOPE_AGENT

constexpr int D = 640, HQ = 32, DH = 128, SK = 32768, WIN = 4096, F = 1728;
constexpr int NSPLIT = 8, KPW = WIN / NSPLIT;
constexpr int NT = 1024, NWAVE = 16;
constexpr int NFG = F / 64;
constexpr unsigned SPIN_CAP = 1u << 16;

constexpr int WS_EPOCH = 0;
constexpr int WS_OG = 64;
constexpr int WS_SG = WS_OG + HQ * NSPLIT * DH;
constexpr int WS_FG = WS_SG + HQ * 16;
constexpr int WS_PG = WS_FG + NFG * NSPLIT * 128;

__device__ __forceinline__ float bf_lo(unsigned w) { return __builtin_bit_cast(float, w << 16); }
__device__ __forceinline__ float bf_hi(unsigned w) { return __builtin_bit_cast(float, w & 0xffff0000u); }
__device__ __forceinline__ unsigned cvtpk(float lo, float hi) {
  unsigned r;
  asm("v_cvt_pk_bf16_f32 %0, %1, %2" : "=v"(r) : "v"(lo), "v"(hi));
  return r;
}
__device__ __forceinline__ float rbf(float x) { return bf_lo(cvtpk(x, x)); }
__device__ __forceinline__ float dot2(unsigned a, unsigned b, float acc) {
  return __builtin_amdgcn_fdot2_f32_bf16(__builtin_bit_cast(bf16x2, a), __builtin_bit_cast(bf16x2, b), acc, false);
}
template <int CTRL> __device__ __forceinline__ float dpp(float x) {
  return __builtin_bit_cast(float, __builtin_amdgcn_mov_dpp(__builtin_bit_cast(int, x), CTRL, 0xf, 0xf, true));
}
constexpr int XOR1 = 0xB1, XOR2 = 0x4E, HMIR = 0x141, ROR8 = 0x128;
__device__ __forceinline__ float row_sum(float x) {
  x += dpp<XOR1>(x); x += dpp<XOR2>(x); x += dpp<HMIR>(x); x += dpp<ROR8>(x);
  return x;
}
__device__ __forceinline__ float fold32(float a, float b) {
  auto t = __builtin_amdgcn_permlane32_swap(__float_as_uint(a), __float_as_uint(b), false, false);
  return __uint_as_float(t[0]) + __uint_as_float(t[1]);
}
__device__ __forceinline__ float fold16(float a, float b) {
  auto t = __builtin_amdgcn_permlane16_swap(__float_as_uint(a), __float_as_uint(b), false, false);
  return __uint_as_float(t[0]) + __uint_as_float(t[1]);
}
__device__ __forceinline__ float xrow_sum(float x) { x = fold16(x, x); return fold32(x, x); }
__device__ __forceinline__ float xrow_max(float x) {
  auto s = __builtin_amdgcn_permlane16_swap(__float_as_uint(x), __float_as_uint(x), false, false);
  x = fmaxf(__uint_as_float(s[0]), __uint_as_float(s[1]));
  auto t = __builtin_amdgcn_permlane32_swap(__float_as_uint(x), __float_as_uint(x), false, false);
  return fmaxf(__uint_as_float(t[0]), __uint_as_float(t[1]));
}
__device__ __forceinline__ float wave_sum(float x) { return xrow_sum(row_sum(x)); }
__device__ __forceinline__ f32x2 fold8(const float (&v)[8]) {
  float t0 = fold32(v[0], v[4]), t1 = fold32(v[1], v[5]), t2 = fold32(v[2], v[6]), t3 = fold32(v[3], v[7]);
  f32x2 u;
  u.x = fold16(t0, t2);
  u.y = fold16(t1, t3);
  return u;
}

__device__ __forceinline__ void put(gu64* g, float v, unsigned tag32, unsigned tag8) {
  const unsigned lo = (__float_as_uint(v) & 0xffffff00u) | tag8;
  __hip_atomic_store(g, ((unsigned long long)tag32 << 32) | lo, RLX_AGENT);
}
template <int N>
__device__ __forceinline__ void gather(gu64* g, const int (&idx)[N], unsigned tag32, unsigned tag8, float (&v)[N]) {
  for (unsigned spins = 0;; ++spins) {
    bool ok = true;
    unsigned long long x[N];
#pragma unroll
    for (int k = 0; k < N; ++k) x[k] = __hip_atomic_load(g + idx[k], RLX_AGENT);
#pragma unroll
    for (int k = 0; k < N; ++k) {
      const unsigned lo = (unsigned)x[k], hi = (unsigned)(x[k] >> 32);
      ok &= (hi == tag32) & ((lo & 0xffu) == tag8);
      v[k] = __uint_as_float(lo & 0xffffff00u);
    }
    if (__all(ok)) return;
    if (spins > SPIN_CAP) {
#pragma unroll
      for (int k = 0; k < N; ++k) v[k] = __uint_as_float(0x7fc00000u);
      return;
    }
    __builtin_amdgcn_s_sleep(1);
  }
}

__device__ __forceinline__ v4u ld_nt(gcv4u* p) { return __builtin_nontemporal_load(p); }
typedef __amdgpu_buffer_rsrc_t rsrc_t;
__device__ __forceinline__ rsrc_t mk(const void* p, unsigned bytes) { return __builtin_amdgcn_make_buffer_rsrc((void*)p, 0, bytes, 0x00020000); }
template <int AUX> __device__ __forceinline__ v4u bld(rsrc_t r, unsigned voff, unsigned soff) { return __builtin_amdgcn_raw_buffer_load_b128(r, voff, soff, AUX); }
#define SB() __builtin_amdgcn_sched_barrier(0)
#define STAMP(k)
#define KV_AUX 2
#define W_AUX 2

__global__ void __launch_bounds__(NT, 4)
fused_decode(const unsigned short* __restrict__ x_, const unsigned short* __restrict__ kc_, const unsigned short* __restrict__ vc_,
             const unsigned short* __restrict__ wq_, const unsigned short* __restrict__ wo_, const unsigned short* __restrict__ wg_,
             const unsigned short* __restrict__ wu_, const unsigned short* __restrict__ wd_, unsigned short* __restrict__ out_,
             unsigned long long* ws_) {
  __shared__ __attribute__((aligned(16))) float s_q[NWAVE * 128];
  __shared__ __attribute__((aligned(16))) float s_gu[NWAVE * 128];
  __shared__ __attribute__((aligned(16))) float s_acc[NWAVE * 128];
  __shared__ __attribute__((aligned(16))) float s_ml[NWAVE * 2];
  __shared__ __attribute__((aligned(16))) float s_o[128];
  __shared__ __attribute__((aligned(16))) float s_h[64];
  __shared__ __attribute__((aligned(16))) float s_p[NWAVE * 128];

  const int b = blockIdx.x, xcd = b & 7, bi = b >> 3;
  const int h = (bi >> 3) * 8 + xcd, s = bi & 7;
  const int tid = threadIdx.x, lane = tid & 63, w = __builtin_amdgcn_readfirstlane(tid >> 6), r = lane >> 4, c = lane & 15;
  const bool ffn = h < NFG;
  gu64* ws = (gu64*)ws_;

  gcu16* x = (gcu16*)x_;
  STAMP(0);
  const rsrc_t rx = mk(x_, D * 2);
  const rsrc_t rk = mk(kc_ + ((size_t)h * SK + (SK - WIN) + s * KPW) * DH, KPW * DH * 2);
  const rsrc_t rv = mk(vc_ + ((size_t)h * SK + (SK - WIN) + s * KPW) * DH, KPW * DH * 2);
  const rsrc_t rq = mk(wq_ + h * DH, D * HQ * DH * 2 - h * DH * 2);
  const rsrc_t rg = mk(wg_ + (size_t)(80 * s) * F + 64 * (ffn ? h : 0), ffn ? 80 * F * 2 - 128 * h : 0);
  const rsrc_t ru = mk(wu_ + (size_t)(80 * s) * F + 64 * (ffn ? h : 0), ffn ? 80 * F * 2 - 128 * h : 0);
  const rsrc_t ro = mk(wo_ + (size_t)(h * DH) * D + 80 * s, DH * D * 2 - 160 * s);
  const rsrc_t rd = mk(wd_ + (size_t)((ffn ? h : 0) * 64) * D + 80 * s, ffn ? 64 * D * 2 - 160 * s : 0);
  unsigned xa[5], xb[5];
  {
    const v4u a4 = bld<0>(rx, 20 * lane, 0);
    xa[4] = __builtin_amdgcn_raw_buffer_load_b32(rx, 20 * lane + 16, 0, 0);
    const v4u b4 = bld<0>(rx, 20 * r, 80 * w);
    xb[4] = __builtin_amdgcn_raw_buffer_load_b32(rx, 20 * r + 16, 80 * w, 0);
#pragma unroll
    for (int i = 0; i < 4; ++i) { xa[i] = a4[i]; xb[i] = b4[i]; }
  }
  const unsigned xg = __builtin_amdgcn_raw_buffer_load_b16(rx, 2 * (lane >> 3), 160 * s + 16 * (w < 10 ? w : 0), 0);
  const unsigned long long ep = __hip_atomic_load(ws + WS_EPOCH, RLX_AGENT);
  SB();
  v4u wqv[10], kv[8], vv[8], gv, uv;
  const unsigned kvoff = r * 256 + 16 * c;
  const unsigned qoff = r * (10 * 8192) + 16 * c;
#pragma unroll
  for (int i = 0; i < 10; ++i) {
    wqv[i] = bld<0>(rq, qoff, (40 * w + i) * 8192);
    if (i < 8) kv[i] = bld<KV_AUX>(rk, kvoff + (i & 3) * 1024, w * 8192 + (i >> 2) * 4096);
    SB();
  }
  {
    const unsigned goff = (lane >> 3) * (F * 2) + 16 * (lane & 7);
    gv = bld<W_AUX>(rg, goff, w * (8 * F * 2));
    uv = bld<W_AUX>(ru, goff, w * (8 * F * 2));
  }
  SB();

  const unsigned e_lo = __builtin_amdgcn_readfirstlane((unsigned)ep), e_hi = __builtin_amdgcn_readfirstlane((unsigned)(ep >> 32));
  unsigned tag32 = (e_lo + 1u) * 0x9E3779B1u; tag32 ^= tag32 >> 15; tag32 *= 0x85EBCA77u; tag32 ^= tag32 >> 13; tag32 ^= 0x5bd1e995u;
  const unsigned tag8 = (((e_hi ^ (e_lo * 0xC2B2AE35u)) * 0x27D4EB2Fu) >> 24) ^ 0x5Au;

  float mean_b, den;
  {
    float xs[10];
#pragma unroll
    for (int i = 0; i < 5; ++i) { xs[2 * i] = bf_lo(xa[i]); xs[2 * i + 1] = bf_hi(xa[i]); }
    float sum = 0.f;
#pragma unroll
    for (int i = 0; i < 10; ++i) sum += xs[i];
    const float mean = wave_sum(sum) * (1.f / D);
    float sq = 0.f;
#pragma unroll
    for (int i = 0; i < 10; ++i) { const float d = xs[i] - mean; sq = fmaf(d, d, sq); }
    const float var = wave_sum(sq) * (1.f / D);
    mean_b = rbf(mean);
    den = rbf(sqrtf(rbf(rbf(var) + 1e-5f)));
  }
  {
    float acc[8];
#pragma unroll
    for (int k = 0; k < 8; ++k) acc[k] = 0.f;
#pragma unroll
    for (int i = 0; i < 10; ++i) {
      const float xv = (i & 1) ? bf_hi(xb[i >> 1]) : bf_lo(xb[i >> 1]);
      const unsigned xn_hi = __float_as_uint(rbf(rbf(xv - mean_b) / den));
      const unsigned xn_lo = xn_hi >> 16;
#pragma unroll
      for (int k = 0; k < 4; ++k) {
        acc[2 * k] = dot2(wqv[i][k], xn_lo, acc[2 * k]);
        acc[2 * k + 1] = dot2(wqv[i][k], xn_hi, acc[2 * k + 1]);
      }
    }
    const f32x2 u = fold8(acc);
    *(f32x2*)(s_q + w * 128 + 8 * c + 2 * r) = u;
  }
  STAMP(1);
#pragma unroll
  for (int i = 0; i < 8; ++i) vv[i] = bld<KV_AUX>(rv, kvoff + (i & 3) * 1024, w * 8192 + (i >> 2) * 4096);
  SB();
  const int rho = 4 * w + r;
  const unsigned ooff = c < 10 ? r * (D * 2) + 16 * c : 0x40000000u;
  v4u woa = bld<0>(ro, ooff, w * (4 * D * 2));
  v4u wob = bld<0>(ro, ooff, w * (4 * D * 2) + 64 * D * 2);
  v4u wdv = bld<0>(rd, ooff, w * (4 * D * 2));
  SB();
  {
    const float xn = rbf(rbf(bf_lo(xg) - mean_b) / den);
    float g[8], u[8];
#pragma unroll
    for (int k = 0; k < 4; ++k) {
      g[2 * k] = xn * bf_lo(gv[k]); g[2 * k + 1] = xn * bf_hi(gv[k]);
      u[2 * k] = xn * bf_lo(uv[k]); u[2 * k + 1] = xn * bf_hi(uv[k]);
    }
    f32x2 gs = fold8(g), us = fold8(u);
    gs.x += dpp<ROR8>(gs.x); gs.y += dpp<ROR8>(gs.y);
    us.x += dpp<ROR8>(us.x); us.y += dpp<ROR8>(us.y);
    *(f32x2*)(s_gu + w * 128 + (c < 8 ? 0 : 64) + 8 * (c & 7) + 2 * r) = (c < 8) ? gs : us;
  }
  __syncthreads();
  STAMP(2);
  unsigned qp[4];
  {
    float q8[8];
#pragma unroll
    for (int k = 0; k < 8; ++k) q8[k] = 0.f;
#pragma unroll
    for (int t = 0; t < 4; ++t) {
      const f32x4 a = *(const f32x4*)(s_q + (4 * r + t) * 128 + 8 * c), bq = *(const f32x4*)(s_q + (4 * r + t) * 128 + 8 * c + 4);
      q8[0] += a.x; q8[1] += a.y; q8[2] += a.z; q8[3] += a.w; q8[4] += bq.x; q8[5] += bq.y; q8[6] += bq.z; q8[7] += bq.w;
    }
#pragma unroll
    for (int k = 0; k < 8; ++k) q8[k] = xrow_sum(q8[k]);
#pragma unroll
    for (int k = 0; k < 4; ++k) qp[k] = cvtpk(q8[2 * k], q8[2 * k + 1]);
  }
  if (ffn && w == 15) {
    float g = 0.f, u = 0.f;
#pragma unroll
    for (int t = 0; t < 10; ++t) { g += s_gu[t * 128 + lane]; u += s_gu[t * 128 + 64 + lane]; }
    gu64* fg = ws + WS_FG + (h * NSPLIT + s) * 128;
    put(fg + lane, g, tag32, tag8);
    put(fg + 64 + lane, u, tag32, tag8);
  }
  float p[8], m_w, l_w;
  {
    constexpr float SCALE = 0.08838834764831845f, LOG2E = 1.4426950408889634f;
    float sc[8];
#pragma unroll
    for (int i = 0; i < 8; ++i) {
      float d = 0.f;
#pragma unroll
      for (int k = 0; k < 4; ++k) d = dot2(kv[i][k], qp[k], d);
      sc[i] = rbf(rbf(row_sum(d)) * SCALE);
    }
    float m = sc[0];
#pragma unroll
    for (int i = 1; i < 8; ++i) m = fmaxf(m, sc[i]);
    m_w = xrow_max(m);
    float l = 0.f;
#pragma unroll
    for (int i = 0; i < 8; ++i) { p[i] = __builtin_amdgcn_exp2f((sc[i] - m_w) * LOG2E); l += p[i]; }
    l_w = xrow_sum(l);
  }
  STAMP(3);
  {
    float acc[8];
#pragma unroll
    for (int k = 0; k < 8; ++k) acc[k] = 0.f;
#pragma unroll
    for (int i = 0; i < 8; ++i)
#pragma unroll
      for (int k = 0; k < 4; ++k) {
        acc[2 * k] = fmaf(p[i], bf_lo(vv[i][k]), acc[2 * k]);
        acc[2 * k + 1] = fmaf(p[i], bf_hi(vv[i][k]), acc[2 * k + 1]);
      }
    const f32x2 u = fold8(acc);
    *(f32x2*)(s_acc + w * 128 + 8 * c + 2 * r) = u;
    if (lane == 0) *(f32x2*)(s_ml + 2 * w) = f32x2{m_w, l_w};
  }
  STAMP(4);
  __syncthreads();
  constexpr float LOG2E = 1.4426950408889634f;
  if (w == 0) {
    f32x2 ml[NWAVE], a[NWAVE];
#pragma unroll
    for (int t = 0; t < NWAVE; ++t) { ml[t] = *(const f32x2*)(s_ml + 2 * t); a[t] = *(const f32x2*)(s_acc + t * 128 + 2 * lane); }
    float M = ml[0].x;
#pragma unroll
    for (int t = 1; t < NWAVE; ++t) M = fmaxf(M, ml[t].x);
    float L = 0.f, o0 = 0.f, o1 = 0.f;
#pragma unroll
    for (int t = 0; t < NWAVE; ++t) {
      const float f = __builtin_amdgcn_exp2f((ml[t].x - M) * LOG2E);
      L = fmaf(f, ml[t].y, L); o0 = fmaf(f, a[t].x, o0); o1 = fmaf(f, a[t].y, o1);
    }
    gu64* og = ws + WS_OG + (h * NSPLIT) * DH;
    gu64* sg = ws + WS_SG + h * 16;
    put(og + s * DH + lane, o0, tag32, tag8);
    put(og + s * DH + 64 + lane, o1, tag32, tag8);
    if (lane < 2) put(sg + 2 * s + lane, lane == 0 ? M : L, tag32, tag8);
    int idx[17];
#pragma unroll
    for (int t = 0; t < 8; ++t) { idx[2 * t] = WS_OG + (h * NSPLIT + t) * DH + lane; idx[2 * t + 1] = idx[2 * t] + 64; }
    idx[16] = WS_SG + h * 16 + (lane & 15);
    float v[17];
    gather<17>(ws, idx, tag32, tag8, v);
    float ms[8], ls[8];
#pragma unroll
    for (int t = 0; t < 8; ++t) {
      ms[t] = __builtin_bit_cast(float, __builtin_amdgcn_readlane(__builtin_bit_cast(int, v[16]), 2 * t));
      ls[t] = __builtin_bit_cast(float, __builtin_amdgcn_readlane(__builtin_bit_cast(int, v[16]), 2 * t + 1));
    }
    float MM = ms[0];
#pragma unroll
    for (int t = 1; t < 8; ++t) MM = fmaxf(MM, ms[t]);
    float LL = 0.f, a0 = 0.f, a1 = 0.f;
#pragma unroll
    for (int t = 0; t < 8; ++t) {
      const float f = __builtin_amdgcn_exp2f((ms[t] - MM) * LOG2E);
      LL = fmaf(f, ls[t], LL); a0 = fmaf(f, v[2 * t], a0); a1 = fmaf(f, v[2 * t + 1], a1);
    }
    const float inv = 1.f / LL;
    *(f32x2*)(s_o + 2 * lane) = f32x2{rbf(a0 * inv), rbf(a1 * inv)};
  } else if (w == 1) {
    float hv = 0.f;
    if (ffn) {
      int idx[16];
#pragma unroll
      for (int t = 0; t < 8; ++t) { idx[2 * t] = WS_FG + (h * NSPLIT + t) * 128 + lane; idx[2 * t + 1] = idx[2 * t] + 64; }
      float v[16];
      gather<16>(ws, idx, tag32, tag8, v);
      float g = 0.f, u = 0.f;
#pragma unroll
      for (int t = 0; t < 8; ++t) { g += v[2 * t]; u += v[2 * t + 1]; }
      g = rbf(g); u = rbf(u);
      const float sg = rbf(1.f / (1.f + __builtin_amdgcn_exp2f(-g * LOG2E)));
      hv = rbf(sg * u);
    }
    s_h[lane] = hv;
  }
  STAMP(5);
  __syncthreads();
  {
    const float oa = s_o[rho], ob = s_o[rho + 64], hh = s_h[rho];
    float acc[8];
#pragma unroll
    for (int k = 0; k < 4; ++k) {
      acc[2 * k] = fmaf(oa, bf_lo(woa[k]), fmaf(ob, bf_lo(wob[k]), hh * bf_lo(wdv[k])));
      acc[2 * k + 1] = fmaf(oa, bf_hi(woa[k]), fmaf(ob, bf_hi(wob[k]), hh * bf_hi(wdv[k])));
    }
    const f32x2 u = fold8(acc);
    *(f32x2*)(s_p + w * 128 + 8 * c + 2 * r) = u;
  }
  __syncthreads();
  STAMP(6);
  if (tid < 80) {
    float a = 0.f;
#pragma unroll
    for (int t = 0; t < NWAVE; ++t) a += s_p[t * 128 + tid];
    put(ws + WS_PG + ((s * 5 + (tid >> 4)) * 32 + h) * 16 + (tid & 15), a, tag32, tag8);
  } else if (w == 2 && h < 5) {
    int idx[8];
#pragma unroll
    for (int t = 0; t < 8; ++t) idx[t] = WS_PG + ((s * 5 + h) * 32 + 8 * r + t) * 16 + c;
    const int col = 80 * s + 16 * h + c;
    const float xv = bf_lo(x[col]);
    float v[8];
    gather<8>(ws, idx, tag32, tag8, v);
    float a = 0.f;
#pragma unroll
    for (int t = 0; t < 8; ++t) a += v[t];
    a = xrow_sum(a);
    if (lane < 16) ((GAS unsigned short*)out_)[col] = (unsigned short)(cvtpk(xv + a, 0.f) & 0xffffu);
    if (b == 0 && lane == 0) __hip_atomic_store(ws + WS_EPOCH, ep + 1ull, RLX_AGENT);
  }
}

extern "C" void kernel_launch(void* const* d_in, const int* in_sizes, int n_in, void* d_out, int out_size, void* d_ws,
                              size_t ws_size, hipStream_t stream) {
  (void)in_sizes; (void)n_in; (void)out_size; (void)ws_size;
  hipLaunchKernelGGL(fused_decode, dim3(256), dim3(NT), 0, stream, (const unsigned short*)d_in[0], (const unsigned short*)d_in[1],
                     (const unsigned short*)d_in[2], (const unsigned short*)d_in[3], (const unsigned short*)d_in[4],
                     (const unsigned short*)d_in[6], (const unsigned short*)d_in[7], (const unsigned short*)d_in[8],
                     (unsigned short*)d_out, (unsigned long long*)d_ws);
}
```

```cpp
#include <hip/hip_runtime.h>
#include <hip/hip_bf16.h>
#include <stdint.h>

#define GAS __attribute__((address_space(1)))
#define LAS __attribute__((address_space(3)))
typedef unsigned v4u __attribute__((ext_vector_type(4)));
typedef unsigned v2u __attribute__((ext_vector_type(2)));
typedef float f32x2 __attribute__((ext_vector_type(2)));
typedef float f32x4 __attribute__((ext_vector_type(4)));
typedef __attribute__((ext_vector_type(2))) __bf16 bf16x2;
typedef GAS unsigned gu32;
typedef GAS unsigned long long gu64;
typedef GAS const unsigned short gcu16;
typedef GAS const unsigned gcu32;
typedef GAS const v4u gcv4u;
#define RLX_AGENT __ATOMIC_RELAXED, __HIP_MEMORY_SCOPE_AGENT
#define RLX_WG __ATOMIC_RELAXED, __HIP_MEMORY_SCOPE_WORKGROUP

constexpr int D = 640, HQ = 32, DH = 128, SK = 32768, WIN = 4096, F = 1728;
constexpr int NSPLIT = 8, KPW = WIN / NSPLIT;
constexpr int NT = 1024, NWAVE = 16;
constexpr int NFG = F / 64;
constexpr unsigned SPIN_CAP = 1u << 16;

constexpr int WS_EPOCH = 0;
constexpr int WS_QG = 64;
constexpr int WS_FG = WS_QG + HQ * NSPLIT * 128;
constexpr int WS_OG = WS_FG + HQ * NSPLIT * 128;
constexpr int WS_SG = WS_OG + HQ * NSPLIT * DH;
constexpr int WS_PG = WS_SG + HQ * 16;

__device__ __forceinline__ float bf_lo(unsigned w) { return __builtin_bit_cast(float, w << 16); }
__device__ __forceinline__ float bf_hi(unsigned w) { return __builtin_bit_cast(float, w & 0xffff0000u); }
__device__ __forceinline__ unsigned cvtpk(float lo, float hi) {
  unsigned r;
  asm("v_cvt_pk_bf16_f32 %0, %1, %2" : "=v"(r) : "v"(lo), "v"(hi));
  return r;
}
__device__ __forceinline__ float rbf(float x) { return bf_lo(cvtpk(x, x)); }
__device__ __forceinline__ float dot2(unsigned a, unsigned b, float acc) {
  return __builtin_amdgcn_fdot2_f32_bf16(__builtin_bit_cast(bf16x2, a), __builtin_bit_cast(bf16x2, b), acc, false);
}
template <int CTRL> __device__ __forceinline__ float dpp(float x) {
  return __builtin_bit_cast(float, __builtin_amdgcn_mov_dpp(__builtin_bit_cast(int, x), CTRL, 0xf, 0xf, true));
}
constexpr int XOR1 = 0xB1, XOR2 = 0x4E, HMIR = 0x141, ROR8 = 0x128;
__device__ __forceinline__ float sum8(float x) { x += dpp<XOR1>(x); x += dpp<XOR2>(x); x += dpp<HMIR>(x); return x; }
__device__ __forceinline__ float max8(float x) { x = fmaxf(x, dpp<XOR1>(x)); x = fmaxf(x, dpp<XOR2>(x)); x = fmaxf(x, dpp<HMIR>(x)); return x; }
__device__ __forceinline__ float row_sum(float x) { x = sum8(x); x += dpp<ROR8>(x); return x; }
__device__ __forceinline__ float fold32(float a, float b) {
  auto t = __builtin_amdgcn_permlane32_swap(__float_as_uint(a), __float_as_uint(b), false, false);
  return __uint_as_float(t[0]) + __uint_as_float(t[1]);
}
__device__ __forceinline__ float fold16(float a, float b) {
  auto t = __builtin_amdgcn_permlane16_swap(__float_as_uint(a), __float_as_uint(b), false, false);
  return __uint_as_float(t[0]) + __uint_as_float(t[1]);
}
__device__ __forceinline__ float xrow_sum(float x) { x = fold16(x, x); return fold32(x, x); }
__device__ __forceinline__ float xrow_max(float x) {
  auto s = __builtin_amdgcn_permlane16_swap(__float_as_uint(x), __float_as_uint(x), false, false);
  x = fmaxf(__uint_as_float(s[0]), __uint_as_float(s[1]));
  auto t = __builtin_amdgcn_permlane32_swap(__float_as_uint(x), __float_as_uint(x), false, false);
  return fmaxf(__uint_as_float(t[0]), __uint_as_float(t[1]));
}
__device__ __forceinline__ float wave_sum(float x) { return xrow_sum(row_sum(x)); }
__device__ __forceinline__ f32x2 fold8(const float (&v)[8]) {
  float t0 = fold32(v[0], v[4]), t1 = fold32(v[1], v[5]), t2 = fold32(v[2], v[6]), t3 = fold32(v[3], v[7]);
  f32x2 u;
  u.x = fold16(t0, t2);
  u.y = fold16(t1, t3);
  return u;
}

__device__ __forceinline__ void put(gu64* g, float v, unsigned tag32, unsigned tag8) {
  const unsigned lo = (__float_as_uint(v) & 0xffffff00u) | tag8;
  __hip_atomic_store(g, ((unsigned long long)tag32 << 32) | lo, RLX_AGENT);
}
template <int N>
__device__ __forceinline__ void gather(gu64* g, const int (&idx)[N], unsigned tag32, unsigned tag8, float (&v)[N]) {
#pragma nounroll
  for (unsigned spins = 0;; ++spins) {
    bool ok = true;
    unsigned long long x[N];
#pragma unroll
    for (int k = 0; k < N; ++k) x[k] = __hip_atomic_load(g + idx[k], RLX_AGENT);
#pragma unroll
    for (int k = 0; k < N; ++k) {
      const unsigned lo = (unsigned)x[k], hi = (unsigned)(x[k] >> 32);
      ok &= (hi == tag32) & ((lo & 0xffu) == tag8);
      v[k] = __uint_as_float(lo & 0xffffff00u);
    }
    if (__all(ok)) return;
    if (spins > SPIN_CAP) {
#pragma unroll
      for (int k = 0; k < N; ++k) v[k] = __uint_as_float(0x7fc00000u);
      return;
    }
    __builtin_amdgcn_s_sleep(1);
  }
}

__device__ __forceinline__ v4u ld_nt(gcv4u* p) { return __builtin_nontemporal_load(p); }
typedef __amdgpu_buffer_rsrc_t rsrc_t;
__device__ __forceinline__ rsrc_t mk(const void* p, unsigned bytes) { return __builtin_amdgcn_make_buffer_rsrc((void*)p, 0, bytes, 0x00020000); }
template <int AUX> __device__ __forceinline__ v4u bld(rsrc_t r, unsigned voff, unsigned soff) { return __builtin_amdgcn_raw_buffer_load_b128(r, voff, soff, AUX); }
#define SB() __builtin_amdgcn_sched_barrier(0)
#define STAMP(k)
constexpr int NTA = 2;

template <int N> struct IC { static constexpr int value = N; };
struct Lds {
  unsigned q[64];
  unsigned xn[40];
  unsigned tag[4];
  unsigned xflag, qflag, mcnt, gcnt;
  float ml[NWAVE * 2];
  float acc[NWAVE * 128];
  float p[NWAVE * 128];
  float gu[10 * 128];
  float o[128];
  float hv[64];
};
template <int N> __device__ __forceinline__ void waitvm() { asm volatile("s_waitcnt vmcnt(%0)" ::"i"(N) : "memory"); }
template <int A, int B> __device__ __forceinline__ void waitvm2(int w) {
  asm volatile("s_cmp_eq_u32 %0, 0\n\ts_cbranch_scc1 1f\n\ts_waitcnt vmcnt(%2)\n1:\n\ts_waitcnt vmcnt(%1)" ::"s"(w), "i"(A), "i"(B) : "scc", "memory");
}
__device__ __forceinline__ void lds_wait(unsigned* word, unsigned want) {
  unsigned spins = 0;
  while (__builtin_amdgcn_readfirstlane(__hip_atomic_load((LAS unsigned*)word, RLX_WG)) != want && ++spins < (1u << 22)) __builtin_amdgcn_s_sleep(2);
  asm volatile("" ::: "memory");
}
#define WND 2
#define WND0 4
#define JX 1
#define JS 14
#define JG 4
#define JQ 10
#define ROTM 7

__global__ void __launch_bounds__(NT, 4)
fused_decode(const unsigned short* __restrict__ x_, const unsigned short* __restrict__ kc_, const unsigned short* __restrict__ vc_,
             const unsigned short* __restrict__ wq_, const unsigned short* __restrict__ wo_, const unsigned short* __restrict__ wg_,
             const unsigned short* __restrict__ wu_, const unsigned short* __restrict__ wd_, unsigned short* __restrict__ out_,
             unsigned long long* ws_) {
  __shared__ __attribute__((aligned(16))) Lds L;
  constexpr float LOG2E = 1.4426950408889634f;
  const int b = blockIdx.x, xcd = b & 7, bi = b >> 3;
  const int h = (bi >> 3) * 8 + xcd, s = bi & 7;
  const int tid = threadIdx.x, lane = tid & 63, w = __builtin_amdgcn_readfirstlane(tid >> 6), r = lane >> 4, c = lane & 15;
  const int rr = lane >> 3;
  const bool ffn = h < NFG;
  gu64* ws = (gu64*)ws_;
  v4u cpf;
  {
    unsigned long long pc;
    unsigned clen;
    asm volatile("s_getpc_b64 %0\n.Lkcode_begin:\n\ts_mov_b32 %1, .Lkcode_end-.Lkcode_begin" : "=s"(pc), "=s"(clen));
    const rsrc_t rc = mk((const void*)pc, clen);
    cpf = bld<0>(rc, tid * 16, 0);
    SB();
  }
  asm volatile("" ::"s"(x_), "s"(kc_), "s"(vc_), "s"(wq_), "s"(wo_), "s"(wg_), "s"(wu_), "s"(wd_), "s"(out_), "s"(ws_));
  STAMP(0);
  const rsrc_t rk = mk(kc_ + ((size_t)h * SK + (SK - WIN) + s * KPW + 32 * w) * DH, 32 * DH * 2);
  const rsrc_t rv = mk(vc_ + ((size_t)h * SK + (SK - WIN) + s * KPW + 32 * w) * DH, 32 * DH * 2);
  const rsrc_t ro = mk(wo_ + (size_t)(h * DH) * D + 80 * s, DH * D * 2 - 160 * s);
  const rsrc_t rd = mk(wd_ + (size_t)((ffn ? h : 0) * 64) * D + 80 * s, ffn ? 64 * D * 2 - 160 * s : 0);
  const unsigned ooff = c < 10 ? r * (D * 2) + 16 * c : 0x40000000u;
  const int rho = 4 * w + r;
  const int rot = (int)(((unsigned)(b * 16 + w) * 2654435761u) >> 16) & ROTM;
  v4u ld[19];
  auto issue = [&](int i) __attribute__((always_inline)) {
    if (i < 8) ld[i] = bld<NTA>(rk, lane * 16, ((i + rot) & 7) * 1024);
    else if (i < 16) ld[i] = bld<NTA>(rv, lane * 16, ((i + rot) & 7) * 1024);
    else if (i == 16) ld[i] = bld<NTA>(ro, ooff, w * (4 * D * 2));
    else if (i == 17) ld[i] = bld<NTA>(ro, ooff, w * (4 * D * 2) + 64 * D * 2);
    else if (i == 18) ld[i] = bld<NTA>(rd, ooff, w * (4 * D * 2));
    SB();
  };
  v4u gv, uv;
  if (w == 0) {
    __builtin_amdgcn_s_setprio(3);
    const rsrc_t rx = mk(x_, D * 2);
    const v4u a4 = bld<0>(rx, 20 * lane, 0);
    const unsigned a1 = __builtin_amdgcn_raw_buffer_load_b32(rx, 20 * lane + 16, 0, 0);
    const v4u b4 = bld<0>(rx, 20 * rr, 160 * s);
    const unsigned b1 = __builtin_amdgcn_raw_buffer_load_b32(rx, 20 * rr + 16, 160 * s, 0);
    const unsigned long long ep = __hip_atomic_load(ws + WS_EPOCH, RLX_AGENT);
    SB();
    constexpr int QS = HQ * DH * 2;
    const rsrc_t rq = mk(wq_ + (size_t)(80 * s) * (HQ * DH) + h * DH, 79 * QS + 256);
    const unsigned woff = rr * 10 * QS + 16 * (lane & 7);
    v4u wv[20];
#pragma unroll
    for (int i = 0; i < 20; ++i) { wv[i] = bld<NTA>(rq, woff + (i & 1) * 128, (i >> 1) * QS); SB(); }
    if (lane == 0) { L.xflag = 0u; L.qflag = 0u; L.mcnt = 0u; L.gcnt = 0u; }
    __syncthreads();
    SB();
    const unsigned e_lo = __builtin_amdgcn_readfirstlane((unsigned)ep), e_hi = __builtin_amdgcn_readfirstlane((unsigned)(ep >> 32));
    unsigned tag32 = (e_lo + 1u) * 0x9E3779B1u; tag32 ^= tag32 >> 15; tag32 *= 0x85EBCA77u; tag32 ^= tag32 >> 13; tag32 ^= 0x5bd1e995u;
    const unsigned tag8 = (((e_hi ^ (e_lo * 0xC2B2AE35u)) * 0x27D4EB2Fu) >> 24) ^ 0x5Au;
    float xs[10];
#pragma unroll
    for (int i = 0; i < 4; ++i) { xs[2 * i] = bf_lo(a4[i]); xs[2 * i + 1] = bf_hi(a4[i]); }
    xs[8] = bf_lo(a1); xs[9] = bf_hi(a1);
    float sum = 0.f;
#pragma unroll
    for (int i = 0; i < 10; ++i) sum += xs[i];
    const float mean = wave_sum(sum) * (1.f / D);
    float sq = 0.f;
#pragma unroll
    for (int i = 0; i < 10; ++i) { const float d = xs[i] - mean; sq = fmaf(d, d, sq); }
    const float var = wave_sum(sq) * (1.f / D);
    const float mean_b = rbf(mean);
    const float rden = 1.f / rbf(sqrtf(rbf(rbf(var) + 1e-5f)));
    const unsigned xb[5] = {b4[0], b4[1], b4[2], b4[3], b1};
    unsigned xp[5];
#pragma unroll
    for (int i = 0; i < 5; ++i) xp[i] = cvtpk(rbf(bf_lo(xb[i]) - mean_b) * rden, rbf(bf_hi(xb[i]) - mean_b) * rden);
    if ((lane & 7) == 0) {
#pragma unroll
      for (int i = 0; i < 5; ++i) L.xn[5 * rr + i] = xp[i];
    }
    if (lane == 0) { L.tag[0] = tag32; L.tag[1] = tag8; L.tag[2] = e_lo; L.tag[3] = e_hi; }
    asm volatile("s_waitcnt lgkmcnt(0)" ::: "memory");
    if (lane == 0) __hip_atomic_store((LAS unsigned*)&L.xflag, 1u, RLX_WG);
    float accA[8], accB[8];
#pragma unroll
    for (int k = 0; k < 8; ++k) { accA[k] = 0.f; accB[k] = 0.f; }
#pragma unroll
    for (int i = 0; i < 10; ++i) {
      const unsigned xlo = (i & 1) ? xp[i >> 1] >> 16 : xp[i >> 1] & 0xffffu, xhi = (i & 1) ? xp[i >> 1] & 0xffff0000u : xp[i >> 1] << 16;
#pragma unroll
      for (int k = 0; k < 4; ++k) {
        accA[2 * k] = dot2(wv[2 * i][k], xlo, accA[2 * k]);
        accA[2 * k + 1] = dot2(wv[2 * i][k], xhi, accA[2 * k + 1]);
        accB[2 * k] = dot2(wv[2 * i + 1][k], xlo, accB[2 * k]);
        accB[2 * k + 1] = dot2(wv[2 * i + 1][k], xhi, accB[2 * k + 1]);
      }
    }
    f32x2 ua = fold8(accA), ub = fold8(accB);
    ua.x += dpp<ROR8>(ua.x); ua.y += dpp<ROR8>(ua.y); ub.x += dpp<ROR8>(ub.x); ub.y += dpp<ROR8>(ub.y);
    const f32x2 u = c < 8 ? ua : ub;
    gu64* pg = ws + WS_QG + (h * NSPLIT + s) * 128;
    put(pg + lane, u.x, tag32, tag8);
    put(pg + 64 + lane, u.y, tag32, tag8);
    STAMP(1);
  } else {
    __syncthreads();
  }
  {
    const unsigned gbytes = __builtin_amdgcn_readfirstlane((ffn && w >= 1 && w <= 10) ? 7 * F * 2 + 128 : 0);
    const size_t goff = (size_t)(80 * s + 8 * ((w + 15) & 15)) * F + 64 * (ffn ? h : 0);
    const bool gon = w >= 1 && w <= 10;
    const rsrc_t rg = mk(wg_ + (gon ? goff : 0), gbytes), ru = mk(wu_ + (gon ? goff : 0), gbytes);
    SB();
    gv = bld<NTA>(rg, rr * (F * 2) + 16 * (lane & 7), 0);
    uv = bld<NTA>(ru, rr * (F * 2) + 16 * (lane & 7), 0);
    SB();
  }
  v4u qp;
  unsigned tag32 = 0, tag8 = 0;
  float sc[8], p[8], acc[8], m_w = 0.f, wt = 0.f;
#pragma unroll
  for (int k = 0; k < 8; ++k) acc[k] = 0.f;
  auto qk = [&](int i) __attribute__((always_inline)) {
    constexpr float SCALE = 0.08838834764831845f;
    float d = 0.f;
#pragma unroll
    for (int k = 0; k < 4; ++k) d = dot2(ld[i][k], qp[k], d);
    sc[i] = rbf(rbf(row_sum(d)) * SCALE);
  };
  auto pv = [&](int i) __attribute__((always_inline)) {
#pragma unroll
    for (int k = 0; k < 4; ++k) {
      acc[2 * k] = fmaf(p[i], bf_lo(ld[8 + i][k]), acc[2 * k]);
      acc[2 * k + 1] = fmaf(p[i], bf_hi(ld[8 + i][k]), acc[2 * k + 1]);
    }
    asm volatile("" : "+v"(acc[0]), "+v"(acc[1]), "+v"(acc[2]), "+v"(acc[3]), "+v"(acc[4]), "+v"(acc[5]), "+v"(acc[6]), "+v"(acc[7]));
  };
#pragma unroll
  for (int i = 0; i < WND; ++i) issue(i);
  auto step = [&](auto J) __attribute__((always_inline)) {
    constexpr int j = decltype(J)::value;
    if constexpr (j + WND < 19) {
      waitvm2<WND0 - 1, WND - 1>(w);
      issue(j + WND);
    }
    if constexpr (j == JX) if (w == 0) {
      const unsigned t32 = __builtin_amdgcn_readfirstlane(L.tag[0]), t8 = __builtin_amdgcn_readfirstlane(L.tag[1]);
      int idx[16];
#pragma unroll
      for (int t = 0; t < 8; ++t) { idx[2 * t] = WS_QG + (h * NSPLIT + t) * 128 + lane; idx[2 * t + 1] = idx[2 * t] + 64; }
      float v[16];
      gather<16>(ws, idx, t32, t8, v);
      float q0 = 0.f, q1 = 0.f;
#pragma unroll
      for (int t = 0; t < 8; ++t) { q0 += v[2 * t]; q1 += v[2 * t + 1]; }
      L.q[4 * c + r] = cvtpk(q0, q1);
      asm volatile("s_waitcnt lgkmcnt(0)" ::: "memory");
      if (lane == 0) __hip_atomic_store((LAS unsigned*)&L.qflag, 1u, RLX_WG);
      STAMP(8);
      SB();
    }
    if constexpr (j == JG) {
      asm volatile("" ::"v"(cpf));
      lds_wait(&L.xflag, 1u);
      tag32 = __builtin_amdgcn_readfirstlane(L.tag[0]); tag8 = __builtin_amdgcn_readfirstlane(L.tag[1]);
      if (ffn && w >= 1 && w <= 10) {
        const unsigned xw = L.xn[4 * (w - 1) + (rr >> 1)];
        const float xn = (rr & 1) ? bf_hi(xw) : bf_lo(xw);
        float g[8], u[8];
#pragma unroll
        for (int k = 0; k < 4; ++k) {
          g[2 * k] = xn * bf_lo(gv[k]); g[2 * k + 1] = xn * bf_hi(gv[k]);
          u[2 * k] = xn * bf_lo(uv[k]); u[2 * k + 1] = xn * bf_hi(uv[k]);
        }
        f32x2 gs = fold8(g), us = fold8(u);
        gs.x += dpp<ROR8>(gs.x); gs.y += dpp<ROR8>(gs.y); us.x += dpp<ROR8>(us.x); us.y += dpp<ROR8>(us.y);
        *(f32x2*)(L.gu + (w - 1) * 128 + 8 * c + 2 * r) = c < 8 ? gs : us;
        asm volatile("s_waitcnt lgkmcnt(0)" ::: "memory");
        unsigned old = 0;
        if (lane == 0) old = __hip_atomic_fetch_add((LAS unsigned*)&L.gcnt, 1u, RLX_WG);
        if (__builtin_amdgcn_readfirstlane(old) == 9u) {
          asm volatile("" ::: "memory");
          float gg = 0.f, uu = 0.f;
#pragma unroll
          for (int t = 0; t < 10; ++t) { gg += L.gu[t * 128 + lane]; uu += L.gu[t * 128 + 64 + lane]; }
          gu64* fg = ws + WS_FG + (h * NSPLIT + s) * 128;
          put(fg + lane, gg, tag32, tag8);
          put(fg + 64 + lane, uu, tag32, tag8);
        }
      }
      SB();
    }
    if constexpr (j == JQ) {
      STAMP(2);
      lds_wait(&L.qflag, 1u);
      qp = *(const v4u*)(L.q + 4 * c);
#pragma unroll
      for (int i = 0; i < 4; ++i) qk(i);
      SB();
    }
    if constexpr (j == JQ + 1) {
#pragma unroll
      for (int i = 4; i < 8; ++i) qk(i);
      float m = sc[0];
#pragma unroll
      for (int i = 1; i < 8; ++i) m = fmaxf(m, sc[i]);
      m_w = xrow_max(m);
      float l = 0.f;
#pragma unroll
      for (int i = 0; i < 8; ++i) { p[i] = __builtin_amdgcn_exp2f((sc[i] - m_w) * LOG2E); l += p[i]; }
      const float l_w = xrow_sum(l);
      unsigned old = 0;
      if (lane == 0) {
        *(f32x2*)(L.ml + 2 * w) = f32x2{m_w, l_w};
        asm volatile("s_waitcnt lgkmcnt(0)" ::: "memory");
        old = __hip_atomic_fetch_add((LAS unsigned*)&L.mcnt, 1u, RLX_WG);
      }
      if (__builtin_amdgcn_readfirstlane(old) == (unsigned)(NWAVE - 1)) {
        asm volatile("" ::: "memory");
        const f32x2 ml = *(const f32x2*)(L.ml + 2 * c);
        const float m8 = max8(ml.x), M = fmaxf(m8, dpp<ROR8>(m8));
        const float Ls = row_sum(__builtin_amdgcn_exp2f((ml.x - M) * LOG2E) * ml.y);
        if (lane < 2) put(ws + WS_SG + h * 16 + lane * 8 + s, lane == 0 ? M : Ls, tag32, tag8);
      }
      STAMP(3);
      SB();
    }
    if constexpr (j == JQ + 2) {
#pragma unroll
      for (int i = 0; i <= j - 8; ++i) pv(i);
      SB();
    }
    if constexpr (j > JQ + 2) { pv(j - 8); SB(); }
    if constexpr (j == JS) {
      lds_wait(&L.mcnt, (unsigned)NWAVE);
      const float mm = L.ml[2 * c], m8 = max8(mm), M = fmaxf(m8, dpp<ROR8>(m8));
      wt = __builtin_amdgcn_exp2f((m_w - M) * LOG2E);
      asm volatile("" : "+v"(wt));
      STAMP(11);
      SB();
    }
  };
  step(IC<0>{});  step(IC<1>{});  step(IC<2>{});  step(IC<3>{});
  step(IC<4>{});  step(IC<5>{});  step(IC<6>{});  step(IC<7>{});
  step(IC<8>{});  step(IC<9>{});  step(IC<10>{});  step(IC<11>{});
  step(IC<12>{});  step(IC<13>{});  step(IC<14>{});  step(IC<15>{});
#pragma unroll
  for (int i = 16 + WND; i < 19; ++i) issue(i);
  {
    const f32x2 u = fold8(acc) * wt;
    *(f32x2*)(L.acc + w * 128 + 8 * c + 2 * r) = u;
  }
  STAMP(4);
  if (w == 0) {
    __syncthreads();
    SB();
    f32x2 a[NWAVE];
#pragma unroll
    for (int t = 0; t < NWAVE; ++t) a[t] = *(const f32x2*)(L.acc + t * 128 + 2 * lane);
#pragma unroll
    for (int st = 1; st < NWAVE; st *= 2)
#pragma unroll
      for (int t = 0; t < NWAVE; t += 2 * st) a[t] += a[t + st];
    const float o0 = a[0].x, o1 = a[0].y;
    gu64* og = ws + WS_OG + (h * NSPLIT + s) * DH;
    put(og + lane, o0, tag32, tag8);
    put(og + 64 + lane, o1, tag32, tag8);
    STAMP(5);
    int idx[17];
#pragma unroll
    for (int t = 0; t < 8; ++t) { idx[2 * t] = WS_OG + (h * NSPLIT + t) * DH + lane; idx[2 * t + 1] = idx[2 * t] + 64; }
    idx[16] = WS_SG + h * 16 + c;
    float v[17];
    gather<17>(ws, idx, tag32, tag8, v);
    STAMP(10);
    const float so = dpp<ROR8>(v[16]);
    const float Ms = c < 8 ? v[16] : so, Lsp = c < 8 ? so : v[16];
    const float MM = max8(Ms);
    const float fs = __builtin_amdgcn_exp2f((Ms - MM) * LOG2E);
    const float ws8 = fs / sum8(fs * Lsp);
    float a0 = 0.f, a1 = 0.f;
#pragma unroll
    for (int t = 0; t < 8; ++t) {
      const float wtt = __builtin_bit_cast(float, __builtin_amdgcn_readlane(__builtin_bit_cast(int, ws8), t));
      a0 = fmaf(wtt, v[2 * t], a0); a1 = fmaf(wtt, v[2 * t + 1], a1);
    }
    *(f32x2*)(L.o + 2 * lane) = f32x2{rbf(a0), rbf(a1)};
  } else {
    __syncthreads();
    STAMP(5);
    if (w == 1) {
      float hv = 0.f;
      if (ffn) {
        int idx[16];
#pragma unroll
        for (int t = 0; t < 8; ++t) { idx[2 * t] = WS_FG + (h * NSPLIT + t) * 128 + lane; idx[2 * t + 1] = idx[2 * t] + 64; }
        float v[16];
        gather<16>(ws, idx, tag32, tag8, v);
        float g = 0.f, u = 0.f;
#pragma unroll
        for (int t = 0; t < 8; ++t) { g += v[2 * t]; u += v[2 * t + 1]; }
        g = rbf(g); u = rbf(u);
        const float sg = rbf(1.f / (1.f + __builtin_amdgcn_exp2f(-g * LOG2E)));
        hv = rbf(sg * u);
      }
      L.hv[lane] = hv;
      STAMP(9);
    }
  }
  __syncthreads();
  {
    const float coef[3] = {L.o[rho], L.o[rho + 64], L.hv[rho]};
    float pa[8];
#pragma unroll
    for (int k = 0; k < 8; ++k) pa[k] = 0.f;
#pragma unroll
    for (int i = 0; i < 3; ++i) {
      const unsigned chi = __float_as_uint(coef[i]), clo = chi >> 16;
#pragma unroll
      for (int k = 0; k < 4; ++k) {
        pa[2 * k] = dot2(ld[16 + i][k], clo, pa[2 * k]);
        pa[2 * k + 1] = dot2(ld[16 + i][k], chi, pa[2 * k + 1]);
      }
    }
    const f32x2 u = fold8(pa);
    *(f32x2*)(L.p + w * 128 + 8 * c + 2 * r) = u;
  }
  __syncthreads();
  STAMP(6);
  if (tid < 80) {
    float a = 0.f;
#pragma unroll
    for (int t = 0; t < NWAVE; ++t) a += L.p[t * 128 + tid];
    put(ws + WS_PG + ((s * 5 + (tid >> 4)) * 32 + h) * 16 + (tid & 15), a, tag32, tag8);
  }
  if (w == 2 && h < 5) {
    int idx[8];
#pragma unroll
    for (int t = 0; t < 8; ++t) idx[t] = WS_PG + ((s * 5 + h) * 32 + 8 * r + t) * 16 + c;
    const int col = 80 * s + 16 * h + c;
    const float xv = bf_lo(((gcu16*)x_)[col]);
    float v[8];
    gather<8>(ws, idx, tag32, tag8, v);
    float a = 0.f;
#pragma unroll
    for (int t = 0; t < 8; ++t) a += v[t];
    a = xrow_sum(a);
    if (lane < 16) ((GAS unsigned short*)out_)[col] = (unsigned short)(cvtpk(xv + a, 0.f) & 0xffffu);
    if (b == 0 && lane == 0) __hip_atomic_store(ws + WS_EPOCH, (((unsigned long long)L.tag[3] << 32) | L.tag[2]) + 1ull, RLX_AGENT);
  }
  asm volatile(".Lkcode_end:" ::: "memory");
}

extern "C" void kernel_launch(void* const* d_in, const int* in_sizes, int n_in, void* d_out, int out_size, void* d_ws,
                              size_t ws_size, hipStream_t stream) {
  (void)in_sizes; (void)n_in; (void)out_size; (void)ws_size;
  hipLaunchKernelGGL(fused_decode, dim3(256), dim3(NT), 0, stream, (const unsigned short*)d_in[0], (const unsigned short*)d_in[1],
                     (const unsigned short*)d_in[2], (const unsigned short*)d_in[3], (const unsigned short*)d_in[4],
                     (const unsigned short*)d_in[6], (const unsigned short*)d_in[7], (const unsigned short*)d_in[8],
                     (unsigned short*)d_out, (unsigned long long*)d_ws);
}
```

```cpp
#define TOUCH 65536
#include <hip/hip_runtime.h>
#include <hip/hip_bf16.h>
#include <stdint.h>

#define GAS __attribute__((address_space(1)))
#define LAS __attribute__((address_space(3)))
typedef unsigned v4u __attribute__((ext_vector_type(4)));
typedef unsigned v2u __attribute__((ext_vector_type(2)));
typedef float f32x2 __attribute__((ext_vector_type(2)));
typedef float f32x4 __attribute__((ext_vector_type(4)));
typedef __attribute__((ext_vector_type(2))) __bf16 bf16x2;
typedef GAS unsigned gu32;
typedef GAS unsigned long long gu64;
typedef GAS const unsigned short gcu16;
typedef GAS const unsigned gcu32;
typedef GAS const v4u gcv4u;
#define RLX_AGENT __ATOMIC_RELAXED, __HIP_MEMORY_SCOPE_AGENT
#define RLX_WG __ATOMIC_RELAXED, __HIP_MEMORY_SCOPE_WORKGROUP

constexpr int D = 640, HQ = 32, DH = 128, SK = 32768, WIN = 4096, F = 1728;
constexpr int NSPLIT = 8, KPW = WIN / NSPLIT;
constexpr int NT = 1024, NWAVE = 16;
constexpr int NFG = F / 64;
constexpr unsigned SPIN_CAP = 1u << 16;

constexpr int WS_EPOCH = 0;
constexpr int WS_QG = 64;
constexpr int WS_FG = WS_QG + HQ * NSPLIT * 128;
constexpr int WS_OG = WS_FG + HQ * NSPLIT * 128;
constexpr int WS_SG = WS_OG + HQ * NSPLIT * DH;
constexpr int WS_PG = WS_SG + HQ * 16;

__device__ __forceinline__ float bf_lo(unsigned w) { return __builtin_bit_cast(float, w << 16); }
__device__ __forceinline__ float bf_hi(unsigned w) { return __builtin_bit_cast(float, w & 0xffff0000u); }
__device__ __forceinline__ unsigned cvtpk(float lo, float hi) {
  unsigned r;
  asm("v_cvt_pk_bf16_f32 %0, %1, %2" : "=v"(r) : "v"(lo), "v"(hi));
  return r;
}
__device__ __forceinline__ float rbf(float x) { return bf_lo(cvtpk(x, x)); }
__device__ __forceinline__ float dot2(unsigned a, unsigned b, float acc) {
  return __builtin_amdgcn_fdot2_f32_bf16(__builtin_bit_cast(bf16x2, a), __builtin_bit_cast(bf16x2, b), acc, false);
}
template <int CTRL> __device__ __forceinline__ float dpp(float x) {
  return __builtin_bit_cast(float, __builtin_amdgcn_mov_dpp(__builtin_bit_cast(int, x), CTRL, 0xf, 0xf, true));
}
constexpr int XOR1 = 0xB1, XOR2 = 0x4E, HMIR = 0x141, ROR8 = 0x128;
__device__ __forceinline__ float sum8(float x) { x += dpp<XOR1>(x); x += dpp<XOR2>(x); x += dpp<HMIR>(x); return x; }
__device__ __forceinline__ float max8(float x) { x = fmaxf(x, dpp<XOR1>(x)); x = fmaxf(x, dpp<XOR2>(x)); x = fmaxf(x, dpp<HMIR>(x)); return x; }
__device__ __forceinline__ float row_sum(float x) { x = sum8(x); x += dpp<ROR8>(x); return x; }
__device__ __forceinline__ float fold32(float a, float b) {
  auto t = __builtin_amdgcn_permlane32_swap(__float_as_uint(a), __float_as_uint(b), false, false);
  return __uint_as_float(t[0]) + __uint_as_float(t[1]);
}
__device__ __forceinline__ float fold16(float a, float b) {
  auto t = __builtin_amdgcn_permlane16_swap(__float_as_uint(a), __float_as_uint(b), false, false);
  return __uint_as_float(t[0]) + __uint_as_float(t[1]);
}
__device__ __forceinline__ float xrow_sum(float x) { x = fold16(x, x); return fold32(x, x); }
__device__ __forceinline__ float xrow_max(float x) {
  auto s = __builtin_amdgcn_permlane16_swap(__float_as_uint(x), __float_as_uint(x), false, false);
  x = fmaxf(__uint_as_float(s[0]), __uint_as_float(s[1]));
  auto t = __builtin_amdgcn_permlane32_swap(__float_as_uint(x), __float_as_uint(x), false, false);
  return fmaxf(__uint_as_float(t[0]), __uint_as_float(t[1]));
}
__device__ __forceinline__ float wave_sum(float x) { return xrow_sum(row_sum(x)); }
__device__ __forceinline__ f32x2 fold8(const float (&v)[8]) {
  float t0 = fold32(v[0], v[4]), t1 = fold32(v[1], v[5]), t2 = fold32(v[2], v[6]), t3 = fold32(v[3], v[7]);
  f32x2 u;
  u.x = fold16(t0, t2);
  u.y = fold16(t1, t3);
  return u;
}

__device__ __forceinline__ void put(gu64* g, float v, unsigned tag32, unsigned tag8) {
  const unsigned lo = (__float_as_uint(v) & 0xffffff00u) | tag8;
  __hip_atomic_store(g, ((unsigned long long)tag32 << 32) | lo, RLX_AGENT);
}
template <int N>
__device__ __forceinline__ void gather(gu64* g, const int (&idx)[N], unsigned tag32, unsigned tag8, float (&v)[N]) {
#pragma nounroll
  for (unsigned spins = 0;; ++spins) {
    bool ok = true;
    unsigned long long x[N];
#pragma unroll
    for (int k = 0; k < N; ++k) x[k] = __hip_atomic_load(g + idx[k], RLX_AGENT);
#pragma unroll
    for (int k = 0; k < N; ++k) {
      const unsigned lo = (unsigned)x[k], hi = (unsigned)(x[k] >> 32);
      ok &= (hi == tag32) & ((lo & 0xffu) == tag8);
      v[k] = __uint_as_float(lo & 0xffffff00u);
    }
    if (__all(ok)) return;
    if (spins > SPIN_CAP) {
#pragma unroll
      for (int k = 0; k < N; ++k) v[k] = __uint_as_float(0x7fc00000u);
      return;
    }
    __builtin_amdgcn_s_sleep(1);
  }
}

__device__ __forceinline__ v4u ld_nt(gcv4u* p) { return __builtin_nontemporal_load(p); }
typedef __amdgpu_buffer_rsrc_t rsrc_t;
__device__ __forceinline__ rsrc_t mk(const void* p, unsigned bytes) { return __builtin_amdgcn_make_buffer_rsrc((void*)p, 0, bytes, 0x00020000); }
template <int AUX> __device__ __forceinline__ v4u bld(rsrc_t r, unsigned voff, unsigned soff) { return __builtin_amdgcn_raw_buffer_load_b128(r, voff, soff, AUX); }
#define SB() __builtin_amdgcn_sched_barrier(0)
#define STAMP(k)
constexpr int NTA = 2;

template <int N> struct IC { static constexpr int value = N; };
struct Lds {
  unsigned q[64];
  unsigned xn[40];
  unsigned tag[4];
  unsigned xflag, qflag, mcnt, gcnt;
  float ml[NWAVE * 2];
  float acc[NWAVE * 128];
  float p[NWAVE * 128];
  float gu[10 * 128];
  float o[128];
  float hv[64];
};
template <int N> __device__ __forceinline__ void waitvm() { asm volatile("s_waitcnt vmcnt(%0)" ::"i"(N) : "memory"); }
template <int A, int B> __device__ __forceinline__ void waitvm2(int w) {
  asm volatile("s_cmp_eq_u32 %0, 0\n\ts_cbranch_scc1 1f\n\ts_waitcnt vmcnt(%2)\n1:\n\ts_waitcnt vmcnt(%1)" ::"s"(w), "i"(A), "i"(B) : "scc", "memory");
}
__device__ __forceinline__ void lds_wait(unsigned* word, unsigned want) {
  unsigned spins = 0;
  while (__builtin_amdgcn_readfirstlane(__hip_atomic_load((LAS unsigned*)word, RLX_WG)) != want && ++spins < (1u << 22)) __builtin_amdgcn_s_sleep(2);
  asm volatile("" ::: "memory");
}
#define WND 2
#define WND0 4
#define JX 1
#define JS 14
#define JG 4
#define JQ 10
#define ROTM 7

__global__ void __launch_bounds__(NT, 4)
fused_decode(const unsigned short* __restrict__ x_, const unsigned short* __restrict__ kc_, const unsigned short* __restrict__ vc_,
             const unsigned short* __restrict__ wq_, const unsigned short* __restrict__ wo_, const unsigned short* __restrict__ wg_,
             const unsigned short* __restrict__ wu_, const unsigned short* __restrict__ wd_, unsigned short* __restrict__ out_,
             unsigned long long* ws_) {
  __shared__ __attribute__((aligned(16))) Lds L;
  constexpr float LOG2E = 1.4426950408889634f;
  const int b = blockIdx.x, xcd = b & 7, bi = b >> 3;
  const int h = (bi >> 3) * 8 + xcd, s = bi & 7;
  const int tid = threadIdx.x, lane = tid & 63, w = __builtin_amdgcn_readfirstlane(tid >> 6), r = lane >> 4, c = lane & 15;
  const int rr = lane >> 3;
  const bool ffn = h < NFG;
  gu64* ws = (gu64*)ws_;
  v4u cpf;
  {
    unsigned long long pc;
    unsigned clen;
    asm volatile("s_getpc_b64 %0\n.Lkcode_begin:\n\ts_mov_b32 %1, .Lkcode_end-.Lkcode_begin" : "=s"(pc), "=s"(clen));
    const rsrc_t rc = mk((const void*)pc, clen);
    cpf = bld<0>(rc, tid * 16, 0);
    SB();
  }
  asm volatile("" ::"s"(x_), "s"(kc_), "s"(vc_), "s"(wq_), "s"(wo_), "s"(wg_), "s"(wu_), "s"(wd_), "s"(out_), "s"(ws_));
  STAMP(0);
  const rsrc_t rk = mk(kc_ + ((size_t)h * SK + (SK - WIN) + s * KPW + 32 * w) * DH, 32 * DH * 2);
  const rsrc_t rv = mk(vc_ + ((size_t)h * SK + (SK - WIN) + s * KPW + 32 * w) * DH, 32 * DH * 2);
  const rsrc_t ro = mk(wo_ + (size_t)(h * DH) * D + 80 * s, DH * D * 2 - 160 * s);
  const rsrc_t rd = mk(wd_ + (size_t)((ffn ? h : 0) * 64) * D + 80 * s, ffn ? 64 * D * 2 - 160 * s : 0);
  const unsigned ooff = c < 10 ? r * (D * 2) + 16 * c : 0x40000000u;
  const int rho = 4 * w + r;
  const int rot = (int)(((unsigned)(b * 16 + w) * 2654435761u) >> 16) & ROTM;
  v4u ld[19];
  auto issue = [&](int i) __attribute__((always_inline)) {
    if (i < 8) ld[i] = bld<NTA>(rk, lane * 16, ((i + rot) & 7) * 1024);
    else if (i < 16) ld[i] = bld<NTA>(rv, lane * 16, ((i + rot) & 7) * 1024);
    else if (i == 16) ld[i] = bld<NTA>(ro, ooff, w * (4 * D * 2));
    else if (i == 17) ld[i] = bld<NTA>(ro, ooff, w * (4 * D * 2) + 64 * D * 2);
    else if (i == 18) ld[i] = bld<NTA>(rd, ooff, w * (4 * D * 2));
    SB();
  };
  v4u gv, uv;
  if (w == 0) {
    __builtin_amdgcn_s_setprio(3);
    const rsrc_t rx = mk(x_, D * 2);
    const v4u a4 = bld<0>(rx, 20 * lane, 0);
    const unsigned a1 = __builtin_amdgcn_raw_buffer_load_b32(rx, 20 * lane + 16, 0, 0);
    const v4u b4 = bld<0>(rx, 20 * rr, 160 * s);
    const unsigned b1 = __builtin_amdgcn_raw_buffer_load_b32(rx, 20 * rr + 16, 160 * s, 0);
    const unsigned long long ep = __hip_atomic_load(ws + WS_EPOCH, RLX_AGENT);
    SB();
    constexpr int QS = HQ * DH * 2;
    const rsrc_t rq = mk(wq_ + (size_t)(80 * s) * (HQ * DH) + h * DH, 79 * QS + 256);
    const unsigned woff = rr * 10 * QS + 16 * (lane & 7);
    v4u wv[20];
#pragma unroll
    for (int i = 0; i < 20; ++i) { wv[i] = bld<NTA>(rq, woff + (i & 1) * 128, (i >> 1) * QS); SB(); }
    if (lane == 0) { L.xflag = 0u; L.qflag = 0u; L.mcnt = 0u; L.gcnt = 0u; }
    __syncthreads();
    SB();
    const unsigned e_lo = __builtin_amdgcn_readfirstlane((unsigned)ep), e_hi = __builtin_amdgcn_readfirstlane((unsigned)(ep >> 32));
    unsigned tag32 = (e_lo + 1u) * 0x9E3779B1u; tag32 ^= tag32 >> 15; tag32 *= 0x85EBCA77u; tag32 ^= tag32 >> 13; tag32 ^= 0x5bd1e995u;
    const unsigned tag8 = (((e_hi ^ (e_lo * 0xC2B2AE35u)) * 0x27D4EB2Fu) >> 24) ^ 0x5Au;
    float xs[10];
#pragma unroll
    for (int i = 0; i < 4; ++i) { xs[2 * i] = bf_lo(a4[i]); xs[2 * i + 1] = bf_hi(a4[i]); }
    xs[8] = bf_lo(a1); xs[9] = bf_hi(a1);
    float sum = 0.f;
#pragma unroll
    for (int i = 0; i < 10; ++i) sum += xs[i];
    const float mean = wave_sum(sum) * (1.f / D);
    float sq = 0.f;
#pragma unroll
    for (int i = 0; i < 10; ++i) { const float d = xs[i] - mean; sq = fmaf(d, d, sq); }
    const float var = wave_sum(sq) * (1.f / D);
    const float mean_b = rbf(mean);
    const float rden = 1.f / rbf(sqrtf(rbf(rbf(var) + 1e-5f)));
    const unsigned xb[5] = {b4[0], b4[1], b4[2], b4[3], b1};
    unsigned xp[5];
#pragma unroll
    for (int i = 0; i < 5; ++i) xp[i] = cvtpk(rbf(bf_lo(xb[i]) - mean_b) * rden, rbf(bf_hi(xb[i]) - mean_b) * rden);
    if ((lane & 7) == 0) {
#pragma unroll
      for (int i = 0; i < 5; ++i) L.xn[5 * rr + i] = xp[i];
    }
    if (lane == 0) { L.tag[0] = tag32; L.tag[1] = tag8; L.tag[2] = e_lo; L.tag[3] = e_hi; }
    asm volatile("s_waitcnt lgkmcnt(0)" ::: "memory");
    if (lane == 0) __hip_atomic_store((LAS unsigned*)&L.xflag, 1u, RLX_WG);
    float accA[8], accB[8];
#pragma unroll
    for (int k = 0; k < 8; ++k) { accA[k] = 0.f; accB[k] = 0.f; }
#pragma unroll
    for (int i = 0; i < 10; ++i) {
      const unsigned xlo = (i & 1) ? xp[i >> 1] >> 16 : xp[i >> 1] & 0xffffu, xhi = (i & 1) ? xp[i >> 1] & 0xffff0000u : xp[i >> 1] << 16;
#pragma unroll
      for (int k = 0; k < 4; ++k) {
        accA[2 * k] = dot2(wv[2 * i][k], xlo, accA[2 * k]);
        accA[2 * k + 1] = dot2(wv[2 * i][k], xhi, accA[2 * k + 1]);
        accB[2 * k] = dot2(wv[2 * i + 1][k], xlo, accB[2 * k]);
        accB[2 * k + 1] = dot2(wv[2 * i + 1][k], xhi, accB[2 * k + 1]);
      }
    }
    f32x2 ua = fold8(accA), ub = fold8(accB);
    ua.x += dpp<ROR8>(ua.x); ua.y += dpp<ROR8>(ua.y); ub.x += dpp<ROR8>(ub.x); ub.y += dpp<ROR8>(ub.y);
    const f32x2 u = c < 8 ? ua : ub;
    gu64* pg = ws + WS_QG + (h * NSPLIT + s) * 128;
    put(pg + lane, u.x, tag32, tag8);
    put(pg + 64 + lane, u.y, tag32, tag8);
    STAMP(1);
  } else {
    __syncthreads();
  }
  {
    const unsigned gbytes = __builtin_amdgcn_readfirstlane((ffn && w >= 1 && w <= 10) ? 7 * F * 2 + 128 : 0);
    const size_t goff = (size_t)(80 * s + 8 * ((w + 15) & 15)) * F + 64 * (ffn ? h : 0);
    const bool gon = w >= 1 && w <= 10;
    const rsrc_t rg = mk(wg_ + (gon ? goff : 0), gbytes), ru = mk(wu_ + (gon ? goff : 0), gbytes);
    SB();
    gv = bld<NTA>(rg, rr * (F * 2) + 16 * (lane & 7), 0);
    uv = bld<NTA>(ru, rr * (F * 2) + 16 * (lane & 7), 0);
    SB();
  }
  unsigned tch;
  {
    const char* tb; unsigned tn;
    if (w == 11) { tb = (const char*)(vc_ + ((size_t)h * SK + (SK - WIN) + s * KPW) * DH); tn = KPW * DH * 2; }
    else if (w == 12) { tb = (const char*)(wo_ + (size_t)(h * DH) * D); tn = DH * D * 2; }
    else if (w == 13) { tb = (const char*)(wd_ + (size_t)((ffn ? h : 0) * 64) * D); tn = 64 * D * 2; }
    else if (w == 14) { tb = (const char*)ws_; tn = TOUCH >= 16384 ? (WS_PG + 8 * 5 * 32 * 16) * 8 : 0; }
    else if (w == 15) { tb = (const char*)out_; tn = D * 2; }
    else { tb = (const char*)x_; tn = 0; }
    const rsrc_t rt = mk(tb, __builtin_amdgcn_readfirstlane(tn));
    tch = __builtin_amdgcn_raw_buffer_load_b32(rt, lane * TOUCH, 0, NTA);
    SB();
  }
  v4u qp;
  unsigned tag32 = 0, tag8 = 0;
  float sc[8], p[8], acc[8], m_w = 0.f, wt = 0.f;
#pragma unroll
  for (int k = 0; k < 8; ++k) acc[k] = 0.f;
  auto qk = [&](int i) __attribute__((always_inline)) {
    constexpr float SCALE = 0.08838834764831845f;
    float d = 0.f;
#pragma unroll
    for (int k = 0; k < 4; ++k) d = dot2(ld[i][k], qp[k], d);
    sc[i] = rbf(rbf(row_sum(d)) * SCALE);
  };
  auto pv = [&](int i) __attribute__((always_inline)) {
#pragma unroll
    for (int k = 0; k < 4; ++k) {
      acc[2 * k] = fmaf(p[i], bf_lo(ld[8 + i][k]), acc[2 * k]);
      acc[2 * k + 1] = fmaf(p[i], bf_hi(ld[8 + i][k]), acc[2 * k + 1]);
    }
    asm volatile("" : "+v"(acc[0]), "+v"(acc[1]), "+v"(acc[2]), "+v"(acc[3]), "+v"(acc[4]), "+v"(acc[5]), "+v"(acc[6]), "+v"(acc[7]));
  };
#pragma unroll
  for (int i = 0; i < WND; ++i) issue(i);
  auto step = [&](auto J) __attribute__((always_inline)) {
    constexpr int j = decltype(J)::value;
    if constexpr (j + WND < 19) {
      waitvm2<WND0 - 1, WND - 1>(w);
      issue(j + WND);
    }
    if constexpr (j == JX) if (w == 0) {
      const unsigned t32 = __builtin_amdgcn_readfirstlane(L.tag[0]), t8 = __builtin_amdgcn_readfirstlane(L.tag[1]);
      int idx[16];
#pragma unroll
      for (int t = 0; t < 8; ++t) { idx[2 * t] = WS_QG + (h * NSPLIT + t) * 128 + lane; idx[2 * t + 1] = idx[2 * t] + 64; }
      float v[16];
      gather<16>(ws, idx, t32, t8, v);
      float q0 = 0.f, q1 = 0.f;
#pragma unroll
      for (int t = 0; t < 8; ++t) { q0 += v[2 * t]; q1 += v[2 * t + 1]; }
      L.q[4 * c + r] = cvtpk(q0, q1);
      asm volatile("s_waitcnt lgkmcnt(0)" ::: "memory");
      if (lane == 0) __hip_atomic_store((LAS unsigned*)&L.qflag, 1u, RLX_WG);
      STAMP(8);
      SB();
    }
    if constexpr (j == JG) {
      asm volatile("" ::"v"(cpf));
      asm volatile("" ::"v"(tch));
      lds_wait(&L.xflag, 1u);
      tag32 = __builtin_amdgcn_readfirstlane(L.tag[0]); tag8 = __builtin_amdgcn_readfirstlane(L.tag[1]);
      if (ffn && w >= 1 && w <= 10) {
        const unsigned xw = L.xn[4 * (w - 1) + (rr >> 1)];
        const float xn = (rr & 1) ? bf_hi(xw) : bf_lo(xw);
        float g[8], u[8];
#pragma unroll
        for (int k = 0; k < 4; ++k) {
          g[2 * k] = xn * bf_lo(gv[k]); g[2 * k + 1] = xn * bf_hi(gv[k]);
          u[2 * k] = xn * bf_lo(uv[k]); u[2 * k + 1] = xn * bf_hi(uv[k]);
        }
        f32x2 gs = fold8(g), us = fold8(u);
        gs.x += dpp<ROR8>(gs.x); gs.y += dpp<ROR8>(gs.y); us.x += dpp<ROR8>(us.x); us.y += dpp<ROR8>(us.y);
        *(f32x2*)(L.gu + (w - 1) * 128 + 8 * c + 2 * r) = c < 8 ? gs : us;
        asm volatile("s_waitcnt lgkmcnt(0)" ::: "memory");
        unsigned old = 0;
        if (lane == 0) old = __hip_atomic_fetch_add((LAS unsigned*)&L.gcnt, 1u, RLX_WG);
        if (__builtin_amdgcn_readfirstlane(old) == 9u) {
          asm volatile("" ::: "memory");
          float gg = 0.f, uu = 0.f;
#pragma unroll
          for (int t = 0; t < 10; ++t) { gg += L.gu[t * 128 + lane]; uu += L.gu[t * 128 + 64 + lane]; }
          gu64* fg = ws + WS_FG + (h * NSPLIT + s) * 128;
          put(fg + lane, gg, tag32, tag8);
          put(fg + 64 + lane, uu, tag32, tag8);
        }
      }
      SB();
    }
    if constexpr (j == JQ) {
      STAMP(2);
      lds_wait(&L.qflag, 1u);
      qp = *(const v4u*)(L.q + 4 * c);
#pragma unroll
      for (int i = 0; i < 4; ++i) qk(i);
      SB();
    }
    if constexpr (j == JQ + 1) {
#pragma unroll
      for (int i = 4; i < 8; ++i) qk(i);
      float m = sc[0];
#pragma unroll
      for (int i = 1; i < 8; ++i) m = fmaxf(m, sc[i]);
      m_w = xrow_max(m);
      float l = 0.f;
#pragma unroll
      for (int i = 0; i < 8; ++i) { p[i] = __builtin_amdgcn_exp2f((sc[i] - m_w) * LOG2E); l += p[i]; }
      const float l_w = xrow_sum(l);
      unsigned old = 0;
      if (lane == 0) {
        *(f32x2*)(L.ml + 2 * w) = f32x2{m_w, l_w};
        asm volatile("s_waitcnt lgkmcnt(0)" ::: "memory");
        old = __hip_atomic_fetch_add((LAS unsigned*)&L.mcnt, 1u, RLX_WG);
      }
      if (__builtin_amdgcn_readfirstlane(old) == (unsigned)(NWAVE - 1)) {
        asm volatile("" ::: "memory");
        const f32x2 ml = *(const f32x2*)(L.ml + 2 * c);
        const float m8 = max8(ml.x), M = fmaxf(m8, dpp<ROR8>(m8));
        const float Ls = row_sum(__builtin_amdgcn_exp2f((ml.x - M) * LOG2E) * ml.y);
        if (lane < 2) put(ws + WS_SG + h * 16 + lane * 8 + s, lane == 0 ? M : Ls, tag32, tag8);
      }
      STAMP(3);
      SB();
    }
    if constexpr (j == JQ + 2) {
#pragma unroll
      for (int i = 0; i <= j - 8; ++i) pv(i);
      SB();
    }
    if constexpr (j > JQ + 2) { pv(j - 8); SB(); }
    if constexpr (j == JS) {
      lds_wait(&L.mcnt, (unsigned)NWAVE);
      const float mm = L.ml[2 * c], m8 = max8(mm), M = fmaxf(m8, dpp<ROR8>(m8));
      wt = __builtin_amdgcn_exp2f((m_w - M) * LOG2E);
      asm volatile("" : "+v"(wt));
      STAMP(11);
      SB();
    }
  };
  step(IC<0>{});  step(IC<1>{});  step(IC<2>{});  step(IC<3>{});
  step(IC<4>{});  step(IC<5>{});  step(IC<6>{});  step(IC<7>{});
  step(IC<8>{});  step(IC<9>{});  step(IC<10>{});  step(IC<11>{});
  step(IC<12>{});  step(IC<13>{});  step(IC<14>{});  step(IC<15>{});
#pragma unroll
  for (int i = 16 + WND; i < 19; ++i) issue(i);
  {
    const f32x2 u = fold8(acc) * wt;
    *(f32x2*)(L.acc + w * 128 + 8 * c + 2 * r) = u;
  }
  STAMP(4);
  if (w == 0) {
    __syncthreads();
    SB();
    f32x2 a[NWAVE];
#pragma unroll
    for (int t = 0; t < NWAVE; ++t) a[t] = *(const f32x2*)(L.acc + t * 128 + 2 * lane);
#pragma unroll
    for (int st = 1; st < NWAVE; st *= 2)
#pragma unroll
      for (int t = 0; t < NWAVE; t += 2 * st) a[t] += a[t + st];
    const float o0 = a[0].x, o1 = a[0].y;
    gu64* og = ws + WS_OG + (h * NSPLIT + s) * DH;
    put(og + lane, o0, tag32, tag8);
    put(og + 64 + lane, o1, tag32, tag8);
    STAMP(5);
    int idx[17];
#pragma unroll
    for (int t = 0; t < 8; ++t) { idx[2 * t] = WS_OG + (h * NSPLIT + t) * DH + lane; idx[2 * t + 1] = idx[2 * t] + 64; }
    idx[16] = WS_SG + h * 16 + c;
    float v[17];
    gather<17>(ws, idx, tag32, tag8, v);
    STAMP(10);
    const float so = dpp<ROR8>(v[16]);
    const float Ms = c < 8 ? v[16] : so, Lsp = c < 8 ? so : v[16];
    const float MM = max8(Ms);
    const float fs = __builtin_amdgcn_exp2f((Ms - MM) * LOG2E);
    const float ws8 = fs / sum8(fs * Lsp);
    float a0 = 0.f, a1 = 0.f;
#pragma unroll
    for (int t = 0; t < 8; ++t) {
      const float wtt = __builtin_bit_cast(float, __builtin_amdgcn_readlane(__builtin_bit_cast(int, ws8), t));
      a0 = fmaf(wtt, v[2 * t], a0); a1 = fmaf(wtt, v[2 * t + 1], a1);
    }
    *(f32x2*)(L.o + 2 * lane) = f32x2{rbf(a0), rbf(a1)};
  } else {
    __syncthreads();
    STAMP(5);
    if (w == 1) {
      float hv = 0.f;
      if (ffn) {
        int idx[16];
#pragma unroll
        for (int t = 0; t < 8; ++t) { idx[2 * t] = WS_FG + (h * NSPLIT + t) * 128 + lane; idx[2 * t + 1] = idx[2 * t] + 64; }
        float v[16];
        gather<16>(ws, idx, tag32, tag8, v);
        float g = 0.f, u = 0.f;
#pragma unroll
        for (int t = 0; t < 8; ++t) { g += v[2 * t]; u += v[2 * t + 1]; }
        g = rbf(g); u = rbf(u);
        const float sg = rbf(1.f / (1.f + __builtin_amdgcn_exp2f(-g * LOG2E)));
        hv = rbf(sg * u);
      }
      L.hv[lane] = hv;
      STAMP(9);
    }
  }
  __syncthreads();
  {
    const float coef[3] = {L.o[rho], L.o[rho + 64], L.hv[rho]};
    float pa[8];
#pragma unroll
    for (int k = 0; k < 8; ++k) pa[k] = 0.f;
#pragma unroll
    for (int i = 0; i < 3; ++i) {
      const unsigned chi = __float_as_uint(coef[i]), clo = chi >> 16;
#pragma unroll
      for (int k = 0; k < 4; ++k) {
        pa[2 * k] = dot2(ld[16 + i][k], clo, pa[2 * k]);
        pa[2 * k + 1] = dot2(ld[16 + i][k], chi, pa[2 * k + 1]);
      }
    }
    const f32x2 u = fold8(pa);
    *(f32x2*)(L.p + w * 128 + 8 * c + 2 * r) = u;
  }
  __syncthreads();
  STAMP(6);
  if (tid < 80) {
    float a = 0.f;
#pragma unroll
    for (int t = 0; t < NWAVE; ++t) a += L.p[t * 128 + tid];
    put(ws + WS_PG + ((s * 5 + (tid >> 4)) * 32 + h) * 16 + (tid & 15), a, tag32, tag8);
  }
  if (w == 2 && h < 5) {
    int idx[8];
#pragma unroll
    for (int t = 0; t < 8; ++t) idx[t] = WS_PG + ((s * 5 + h) * 32 + 8 * r + t) * 16 + c;
    const int col = 80 * s + 16 * h + c;
    const float xv = bf_lo(((gcu16*)x_)[col]);
    float v[8];
    gather<8>(ws, idx, tag32, tag8, v);
    float a = 0.f;
#pragma unroll
    for (int t = 0; t < 8; ++t) a += v[t];
    a = xrow_sum(a);
    if (lane < 16) ((GAS unsigned short*)out_)[col] = (unsigned short)(cvtpk(xv + a, 0.f) & 0xffffu);
    if (b == 0 && lane == 0) __hip_atomic_store(ws + WS_EPOCH, (((unsigned long long)L.tag[3] << 32) | L.tag[2]) + 1ull, RLX_AGENT);
  }
  asm volatile(".Lkcode_end:" ::: "memory");
}

extern "C" void kernel_launch(void* const* d_in, const int* in_sizes, int n_in, void* d_out, int out_size, void* d_ws,
                              size_t ws_size, hipStream_t stream) {
  (void)in_sizes; (void)n_in; (void)out_size; (void)ws_size;
  hipLaunchKernelGGL(fused_decode, dim3(256), dim3(NT), 0, stream, (const unsigned short*)d_in[0], (const unsigned short*)d_in[1],
                     (const unsigned short*)d_in[2], (const unsigned short*)d_in[3], (const unsigned short*)d_in[4],
                     (const unsigned short*)d_in[6], (const unsigned short*)d_in[7], (const unsigned short*)d_in[8],
                     (unsigned short*)d_out, (unsigned long long*)d_ws);
}
```

```cpp
#define PPOINT 0
#define PMULT 0
#include <hip/hip_runtime.h>
#include <hip/hip_bf16.h>
#include <stdint.h>

#define GAS __attribute__((address_space(1)))
#define LAS __attribute__((address_space(3)))
typedef unsigned v4u __attribute__((ext_vector_type(4)));
typedef unsigned v2u __attribute__((ext_vector_type(2)));
typedef float f32x2 __attribute__((ext_vector_type(2)));
typedef float f32x4 __attribute__((ext_vector_type(4)));
typedef __attribute__((ext_vector_type(2))) __bf16 bf16x2;
typedef GAS unsigned gu32;
typedef GAS unsigned long long gu64;
typedef GAS const unsigned short gcu16;
typedef GAS const unsigned gcu32;
typedef GAS const v4u gcv4u;
#define RLX_AGENT __ATOMIC_RELAXED, __HIP_MEMORY_SCOPE_AGENT
#define RLX_WG __ATOMIC_RELAXED, __HIP_MEMORY_SCOPE_WORKGROUP

constexpr int D = 640, HQ = 32, DH = 128, SK = 32768, WIN = 4096, F = 1728;
constexpr int NSPLIT = 8, KPW = WIN / NSPLIT;
constexpr int NT = 1024, NWAVE = 16;
constexpr int NFG = F / 64;
constexpr unsigned SPIN_CAP = 1u << 16;

constexpr int WS_EPOCH = 0;
constexpr int WS_QG = 64;
constexpr int WS_FG = WS_QG + HQ * NSPLIT * 128;
constexpr int WS_OG = WS_FG + HQ * NSPLIT * 128;
constexpr int WS_SG = WS_OG + HQ * NSPLIT * DH;
constexpr int WS_PG = WS_SG + HQ * 16;
constexpr int WS_TM = WS_PG + 8 * 5 * 32 * 16;

__device__ __forceinline__ float bf_lo(unsigned w) { return __builtin_bit_cast(float, w << 16); }
__device__ __forceinline__ float bf_hi(unsigned w) { return __builtin_bit_cast(float, w & 0xffff0000u); }
__device__ __forceinline__ unsigned cvtpk(float lo, float hi) {
  unsigned r;
  asm("v_cvt_pk_bf16_f32 %0, %1, %2" : "=v"(r) : "v"(lo), "v"(hi));
  return r;
}
__device__ __forceinline__ float rbf(float x) { return bf_lo(cvtpk(x, x)); }
__device__ __forceinline__ float dot2(unsigned a, unsigned b, float acc) {
  return __builtin_amdgcn_fdot2_f32_bf16(__builtin_bit_cast(bf16x2, a), __builtin_bit_cast(bf16x2, b), acc, false);
}
template <int CTRL> __device__ __forceinline__ float dpp(float x) {
  return __builtin_bit_cast(float, __builtin_amdgcn_mov_dpp(__builtin_bit_cast(int, x), CTRL, 0xf, 0xf, true));
}
constexpr int XOR1 = 0xB1, XOR2 = 0x4E, HMIR = 0x141, ROR8 = 0x128;
__device__ __forceinline__ float sum8(float x) { x += dpp<XOR1>(x); x += dpp<XOR2>(x); x += dpp<HMIR>(x); return x; }
__device__ __forceinline__ float max8(float x) { x = fmaxf(x, dpp<XOR1>(x)); x = fmaxf(x, dpp<XOR2>(x)); x = fmaxf(x, dpp<HMIR>(x)); return x; }
__device__ __forceinline__ float row_sum(float x) { x = sum8(x); x += dpp<ROR8>(x); return x; }
__device__ __forceinline__ float fold32(float a, float b) {
  auto t = __builtin_amdgcn_permlane32_swap(__float_as_uint(a), __float_as_uint(b), false, false);
  return __uint_as_float(t[0]) + __uint_as_float(t[1]);
}
__device__ __forceinline__ float fold16(float a, float b) {
  auto t = __builtin_amdgcn_permlane16_swap(__float_as_uint(a), __float_as_uint(b), false, false);
  return __uint_as_float(t[0]) + __uint_as_float(t[1]);
}
__device__ __forceinline__ float xrow_sum(float x) { x = fold16(x, x); return fold32(x, x); }
__device__ __forceinline__ float xrow_max(float x) {
  auto s = __builtin_amdgcn_permlane16_swap(__float_as_uint(x), __float_as_uint(x), false, false);
  x = fmaxf(__uint_as_float(s[0]), __uint_as_float(s[1]));
  auto t = __builtin_amdgcn_permlane32_swap(__float_as_uint(x), __float_as_uint(x), false, false);
  return fmaxf(__uint_as_float(t[0]), __uint_as_float(t[1]));
}
__device__ __forceinline__ float wave_sum(float x) { return xrow_sum(row_sum(x)); }
__device__ __forceinline__ f32x2 fold8(const float (&v)[8]) {
  float t0 = fold32(v[0], v[4]), t1 = fold32(v[1], v[5]), t2 = fold32(v[2], v[6]), t3 = fold32(v[3], v[7]);
  f32x2 u;
  u.x = fold16(t0, t2);
  u.y = fold16(t1, t3);
  return u;
}

__device__ __forceinline__ void put(gu64* g, float v, unsigned tag32, unsigned tag8) {
  const unsigned lo = (__float_as_uint(v) & 0xffffff00u) | tag8;
  __hip_atomic_store(g, ((unsigned long long)tag32 << 32) | lo, RLX_AGENT);
}
template <int N>
__device__ __forceinline__ void gather(gu64* g, const int (&idx)[N], unsigned tag32, unsigned tag8, float (&v)[N]) {
#pragma nounroll
  for (unsigned spins = 0;; ++spins) {
    bool ok = true;
    unsigned long long x[N];
#pragma unroll
    for (int k = 0; k < N; ++k) x[k] = __hip_atomic_load(g + idx[k], RLX_AGENT);
#pragma unroll
    for (int k = 0; k < N; ++k) {
      const unsigned lo = (unsigned)x[k], hi = (unsigned)(x[k] >> 32);
      ok &= (hi == tag32) & ((lo & 0xffu) == tag8);
      v[k] = __uint_as_float(lo & 0xffffff00u);
    }
    if (__all(ok)) return;
    if (spins > SPIN_CAP) {
#pragma unroll
      for (int k = 0; k < N; ++k) v[k] = __uint_as_float(0x7fc00000u);
      return;
    }
    __builtin_amdgcn_s_sleep(1);
  }
}

__device__ __forceinline__ v4u ld_nt(gcv4u* p) { return __builtin_nontemporal_load(p); }
typedef __amdgpu_buffer_rsrc_t rsrc_t;
__device__ __forceinline__ rsrc_t mk(const void* p, unsigned bytes) { return __builtin_amdgcn_make_buffer_rsrc((void*)p, 0, bytes, 0x00020000); }
template <int AUX> __device__ __forceinline__ v4u bld(rsrc_t r, unsigned voff, unsigned soff) { return __builtin_amdgcn_raw_buffer_load_b128(r, voff, soff, AUX); }
#define SB() __builtin_amdgcn_sched_barrier(0)
#define STAMP(k)
constexpr int NTA = 2;

template <int N> struct IC { static constexpr int value = N; };
struct Lds {
  unsigned q[64];
  unsigned xn[40];
  unsigned tag[4];
  unsigned xflag, qflag, mcnt, gcnt;
  float ml[NWAVE * 2];
  float acc[NWAVE * 128];
  float p[NWAVE * 128];
  float gu[10 * 128];
  float o[128];
  float hv[64];
};
template <int N> __device__ __forceinline__ void waitvm() { asm volatile("s_waitcnt vmcnt(%0)" ::"i"(N) : "memory"); }
template <int A, int B> __device__ __forceinline__ void waitvm2(int w) {
  asm volatile("s_cmp_eq_u32 %0, 0\n\ts_cbranch_scc1 1f\n\ts_waitcnt vmcnt(%2)\n1:\n\ts_waitcnt vmcnt(%1)" ::"s"(w), "i"(A), "i"(B) : "scc", "memory");
}
__device__ __forceinline__ void lds_wait(unsigned* word, unsigned want) {
  unsigned spins = 0;
  while (__builtin_amdgcn_readfirstlane(__hip_atomic_load((LAS unsigned*)word, RLX_WG)) != want && ++spins < (1u << 22)) __builtin_amdgcn_s_sleep(2);
  asm volatile("" ::: "memory");
}
#define WND 2
#define WND0 4
#define JX 1
#define JS 14
#define JG 4
#define JQ 10
#define ROTM 7

__global__ void __launch_bounds__(NT, 4)
fused_decode(const unsigned short* __restrict__ x_, const unsigned short* __restrict__ kc_, const unsigned short* __restrict__ vc_,
             const unsigned short* __restrict__ wq_, const unsigned short* __restrict__ wo_, const unsigned short* __restrict__ wg_,
             const unsigned short* __restrict__ wu_, const unsigned short* __restrict__ wd_, unsigned short* __restrict__ out_,
             unsigned long long* ws_) {
  __shared__ __attribute__((aligned(16))) Lds L;
  constexpr float LOG2E = 1.4426950408889634f;
  const int b = blockIdx.x, xcd = b & 7, bi = b >> 3;
  const int h = (bi >> 3) * 8 + xcd, s = bi & 7;
  const int tid = threadIdx.x, lane = tid & 63, w = __builtin_amdgcn_readfirstlane(tid >> 6), r = lane >> 4, c = lane & 15;
  const int rr = lane >> 3;
  const bool ffn = h < NFG;
  gu64* ws = (gu64*)ws_;
  v4u cpf;
  {
    unsigned long long pc;
    unsigned clen;
    asm volatile("s_getpc_b64 %0\n.Lkcode_begin:\n\ts_mov_b32 %1, .Lkcode_end-.Lkcode_begin" : "=s"(pc), "=s"(clen));
    const rsrc_t rc = mk((const void*)pc, clen);
    cpf = bld<0>(rc, tid * 16, 0);
    SB();
  }
  asm volatile("" ::"s"(x_), "s"(kc_), "s"(vc_), "s"(wq_), "s"(wo_), "s"(wg_), "s"(wu_), "s"(wd_), "s"(out_), "s"(ws_));
  STAMP(0);
  unsigned long long pt0, pt1 = 0;
  asm volatile("s_memrealtime %0\n\ts_waitcnt lgkmcnt(0)" : "=s"(pt0) :: "memory");
#define PPT(k) if (PPOINT == k) asm volatile("s_memrealtime %0\n\ts_waitcnt lgkmcnt(0)" : "=s"(pt1) :: "memory")
  PPT(0);
  const rsrc_t rk = mk(kc_ + ((size_t)h * SK + (SK - WIN) + s * KPW + 32 * w) * DH, 32 * DH * 2);
  const rsrc_t rv = mk(vc_ + ((size_t)h * SK + (SK - WIN) + s * KPW + 32 * w) * DH, 32 * DH * 2);
  const rsrc_t ro = mk(wo_ + (size_t)(h * DH) * D + 80 * s, DH * D * 2 - 160 * s);
  const rsrc_t rd = mk(wd_ + (size_t)((ffn ? h : 0) * 64) * D + 80 * s, ffn ? 64 * D * 2 - 160 * s : 0);
  const unsigned ooff = c < 10 ? r * (D * 2) + 16 * c : 0x40000000u;
  const int rho = 4 * w + r;
  const int rot = (int)(((unsigned)(b * 16 + w) * 2654435761u) >> 16) & ROTM;
  v4u ld[19];
  auto issue = [&](int i) __attribute__((always_inline)) {
    if (i < 8) ld[i] = bld<NTA>(rk, lane * 16, ((i + rot) & 7) * 1024);
    else if (i < 16) ld[i] = bld<NTA>(rv, lane * 16, ((i + rot) & 7) * 1024);
    else if (i == 16) ld[i] = bld<NTA>(ro, ooff, w * (4 * D * 2));
    else if (i == 17) ld[i] = bld<NTA>(ro, ooff, w * (4 * D * 2) + 64 * D * 2);
    else if (i == 18) ld[i] = bld<NTA>(rd, ooff, w * (4 * D * 2));
    SB();
  };
  v4u gv, uv;
  if (w == 0) {
    __builtin_amdgcn_s_setprio(3);
    const rsrc_t rx = mk(x_, D * 2);
    const v4u a4 = bld<0>(rx, 20 * lane, 0);
    const unsigned a1 = __builtin_amdgcn_raw_buffer_load_b32(rx, 20 * lane + 16, 0, 0);
    const v4u b4 = bld<0>(rx, 20 * rr, 160 * s);
    const unsigned b1 = __builtin_amdgcn_raw_buffer_load_b32(rx, 20 * rr + 16, 160 * s, 0);
    const unsigned long long ep = __hip_atomic_load(ws + WS_EPOCH, RLX_AGENT);
    SB();
    constexpr int QS = HQ * DH * 2;
    const rsrc_t rq = mk(wq_ + (size_t)(80 * s) * (HQ * DH) + h * DH, 79 * QS + 256);
    const unsigned woff = rr * 10 * QS + 16 * (lane & 7);
    v4u wv[20];
#pragma unroll
    for (int i = 0; i < 20; ++i) { wv[i] = bld<NTA>(rq, woff + (i & 1) * 128, (i >> 1) * QS); SB(); }
    if (lane == 0) { L.xflag = 0u; L.qflag = 0u; L.mcnt = 0u; L.gcnt = 0u; }
    __syncthreads();
    SB();
    const unsigned e_lo = __builtin_amdgcn_readfirstlane((unsigned)ep), e_hi = __builtin_amdgcn_readfirstlane((unsigned)(ep >> 32));
    unsigned tag32 = (e_lo + 1u) * 0x9E3779B1u; tag32 ^= tag32 >> 15; tag32 *= 0x85EBCA77u; tag32 ^= tag32 >> 13; tag32 ^= 0x5bd1e995u;
    const unsigned tag8 = (((e_hi ^ (e_lo * 0xC2B2AE35u)) * 0x27D4EB2Fu) >> 24) ^ 0x5Au;
    float xs[10];
#pragma unroll
    for (int i = 0; i < 4; ++i) { xs[2 * i] = bf_lo(a4[i]); xs[2 * i + 1] = bf_hi(a4[i]); }
    xs[8] = bf_lo(a1); xs[9] = bf_hi(a1);
    float sum = 0.f;
#pragma unroll
    for (int i = 0; i < 10; ++i) sum += xs[i];
    const float mean = wave_sum(sum) * (1.f / D);
    float sq = 0.f;
#pragma unroll
    for (int i = 0; i < 10; ++i) { const float d = xs[i] - mean; sq = fmaf(d, d, sq); }
    const float var = wave_sum(sq) * (1.f / D);
    const float mean_b = rbf(mean);
    const float rden = 1.f / rbf(sqrtf(rbf(rbf(var) + 1e-5f)));
    const unsigned xb[5] = {b4[0], b4[1], b4[2], b4[3], b1};
    unsigned xp[5];
#pragma unroll
    for (int i = 0; i < 5; ++i) xp[i] = cvtpk(rbf(bf_lo(xb[i]) - mean_b) * rden, rbf(bf_hi(xb[i]) - mean_b) * rden);
    if ((lane & 7) == 0) {
#pragma unroll
      for (int i = 0; i < 5; ++i) L.xn[5 * rr + i] = xp[i];
    }
    if (lane == 0) { L.tag[0] = tag32; L.tag[1] = tag8; L.tag[2] = e_lo; L.tag[3] = e_hi; }
    asm volatile("s_waitcnt lgkmcnt(0)" ::: "memory");
    if (lane == 0) __hip_atomic_store((LAS unsigned*)&L.xflag, 1u, RLX_WG);
    float accA[8], accB[8];
#pragma unroll
    for (int k = 0; k < 8; ++k) { accA[k] = 0.f; accB[k] = 0.f; }
#pragma unroll
    for (int i = 0; i < 10; ++i) {
      const unsigned xlo = (i & 1) ? xp[i >> 1] >> 16 : xp[i >> 1] & 0xffffu, xhi = (i & 1) ? xp[i >> 1] & 0xffff0000u : xp[i >> 1] << 16;
#pragma unroll
      for (int k = 0; k < 4; ++k) {
        accA[2 * k] = dot2(wv[2 * i][k], xlo, accA[2 * k]);
        accA[2 * k + 1] = dot2(wv[2 * i][k], xhi, accA[2 * k + 1]);
        accB[2 * k] = dot2(wv[2 * i + 1][k], xlo, accB[2 * k]);
        accB[2 * k + 1] = dot2(wv[2 * i + 1][k], xhi, accB[2 * k + 1]);
      }
    }
    f32x2 ua = fold8(accA), ub = fold8(accB);
    ua.x += dpp<ROR8>(ua.x); ua.y += dpp<ROR8>(ua.y); ub.x += dpp<ROR8>(ub.x); ub.y += dpp<ROR8>(ub.y);
    const f32x2 u = c < 8 ? ua : ub;
    gu64* pg = ws + WS_QG + (h * NSPLIT + s) * 128;
    put(pg + lane, u.x, tag32, tag8);
    put(pg + 64 + lane, u.y, tag32, tag8);
    STAMP(1);
    PPT(1);
  } else {
    __syncthreads();
  }
  {
    const unsigned gbytes = __builtin_amdgcn_readfirstlane((ffn && w >= 1 && w <= 10) ? 7 * F * 2 + 128 : 0);
    const size_t goff = (size_t)(80 * s + 8 * ((w + 15) & 15)) * F + 64 * (ffn ? h : 0);
    const bool gon = w >= 1 && w <= 10;
    const rsrc_t rg = mk(wg_ + (gon ? goff : 0), gbytes), ru = mk(wu_ + (gon ? goff : 0), gbytes);
    SB();
    gv = bld<NTA>(rg, rr * (F * 2) + 16 * (lane & 7), 0);
    uv = bld<NTA>(ru, rr * (F * 2) + 16 * (lane & 7), 0);
    SB();
  }
  v4u qp;
  unsigned tag32 = 0, tag8 = 0;
  float sc[8], p[8], acc[8], m_w = 0.f, wt = 0.f;
#pragma unroll
  for (int k = 0; k < 8; ++k) acc[k] = 0.f;
  auto qk = [&](int i) __attribute__((always_inline)) {
    constexpr float SCALE = 0.08838834764831845f;
    float d = 0.f;
#pragma unroll
    for (int k = 0; k < 4; ++k) d = dot2(ld[i][k], qp[k], d);
    sc[i] = rbf(rbf(row_sum(d)) * SCALE);
  };
  auto pv = [&](int i) __attribute__((always_inline)) {
#pragma unroll
    for (int k = 0; k < 4; ++k) {
      acc[2 * k] = fmaf(p[i], bf_lo(ld[8 + i][k]), acc[2 * k]);
      acc[2 * k + 1] = fmaf(p[i], bf_hi(ld[8 + i][k]), acc[2 * k + 1]);
    }
    asm volatile("" : "+v"(acc[0]), "+v"(acc[1]), "+v"(acc[2]), "+v"(acc[3]), "+v"(acc[4]), "+v"(acc[5]), "+v"(acc[6]), "+v"(acc[7]));
  };
#pragma unroll
  for (int i = 0; i < WND; ++i) issue(i);
  auto step = [&](auto J) __attribute__((always_inline)) {
    constexpr int j = decltype(J)::value;
    if constexpr (j + WND < 19) {
      waitvm2<WND0 - 1, WND - 1>(w);
      issue(j + WND);
    }
    if constexpr (j == JX) if (w == 0) {
      const unsigned t32 = __builtin_amdgcn_readfirstlane(L.tag[0]), t8 = __builtin_amdgcn_readfirstlane(L.tag[1]);
      int idx[16];
#pragma unroll
      for (int t = 0; t < 8; ++t) { idx[2 * t] = WS_QG + (h * NSPLIT + t) * 128 + lane; idx[2 * t + 1] = idx[2 * t] + 64; }
      float v[16];
      gather<16>(ws, idx, t32, t8, v);
      float q0 = 0.f, q1 = 0.f;
#pragma unroll
      for (int t = 0; t < 8; ++t) { q0 += v[2 * t]; q1 += v[2 * t + 1]; }
      L.q[4 * c + r] = cvtpk(q0, q1);
      asm volatile("s_waitcnt lgkmcnt(0)" ::: "memory");
      if (lane == 0) __hip_atomic_store((LAS unsigned*)&L.qflag, 1u, RLX_WG);
      STAMP(8);
      PPT(8);
      SB();
    }
    if constexpr (j == JG) {
      asm volatile("" ::"v"(cpf));
      lds_wait(&L.xflag, 1u);
      tag32 = __builtin_amdgcn_readfirstlane(L.tag[0]); tag8 = __builtin_amdgcn_readfirstlane(L.tag[1]);
      if (ffn && w >= 1 && w <= 10) {
        const unsigned xw = L.xn[4 * (w - 1) + (rr >> 1)];
        const float xn = (rr & 1) ? bf_hi(xw) : bf_lo(xw);
        float g[8], u[8];
#pragma unroll
        for (int k = 0; k < 4; ++k) {
          g[2 * k] = xn * bf_lo(gv[k]); g[2 * k + 1] = xn * bf_hi(gv[k]);
          u[2 * k] = xn * bf_lo(uv[k]); u[2 * k + 1] = xn * bf_hi(uv[k]);
        }
        f32x2 gs = fold8(g), us = fold8(u);
        gs.x += dpp<ROR8>(gs.x); gs.y += dpp<ROR8>(gs.y); us.x += dpp<ROR8>(us.x); us.y += dpp<ROR8>(us.y);
        *(f32x2*)(L.gu + (w - 1) * 128 + 8 * c + 2 * r) = c < 8 ? gs : us;
        asm volatile("s_waitcnt lgkmcnt(0)" ::: "memory");
        unsigned old = 0;
        if (lane == 0) old = __hip_atomic_fetch_add((LAS unsigned*)&L.gcnt, 1u, RLX_WG);
        if (__builtin_amdgcn_readfirstlane(old) == 9u) {
          asm volatile("" ::: "memory");
          float gg = 0.f, uu = 0.f;
#pragma unroll
          for (int t = 0; t < 10; ++t) { gg += L.gu[t * 128 + lane]; uu += L.gu[t * 128 + 64 + lane]; }
          gu64* fg = ws + WS_FG + (h * NSPLIT + s) * 128;
          put(fg + lane, gg, tag32, tag8);
          put(fg + 64 + lane, uu, tag32, tag8);
        }
      }
      SB();
    }
    if constexpr (j == JQ) {
      STAMP(2);
      lds_wait(&L.qflag, 1u);
      qp = *(const v4u*)(L.q + 4 * c);
#pragma unroll
      for (int i = 0; i < 4; ++i) qk(i);
      SB();
    }
    if constexpr (j == JQ + 1) {
#pragma unroll
      for (int i = 4; i < 8; ++i) qk(i);
      float m = sc[0];
#pragma unroll
      for (int i = 1; i < 8; ++i) m = fmaxf(m, sc[i]);
      m_w = xrow_max(m);
      float l = 0.f;
#pragma unroll
      for (int i = 0; i < 8; ++i) { p[i] = __builtin_amdgcn_exp2f((sc[i] - m_w) * LOG2E); l += p[i]; }
      const float l_w = xrow_sum(l);
      unsigned old = 0;
      if (lane == 0) {
        *(f32x2*)(L.ml + 2 * w) = f32x2{m_w, l_w};
        asm volatile("s_waitcnt lgkmcnt(0)" ::: "memory");
        old = __hip_atomic_fetch_add((LAS unsigned*)&L.mcnt, 1u, RLX_WG);
      }
      if (__builtin_amdgcn_readfirstlane(old) == (unsigned)(NWAVE - 1)) {
        asm volatile("" ::: "memory");
        const f32x2 ml = *(const f32x2*)(L.ml + 2 * c);
        const float m8 = max8(ml.x), M = fmaxf(m8, dpp<ROR8>(m8));
        const float Ls = row_sum(__builtin_amdgcn_exp2f((ml.x - M) * LOG2E) * ml.y);
        if (lane < 2) put(ws + WS_SG + h * 16 + lane * 8 + s, lane == 0 ? M : Ls, tag32, tag8);
      }
      STAMP(3);
      SB();
    }
    if constexpr (j == JQ + 2) {
#pragma unroll
      for (int i = 0; i <= j - 8; ++i) pv(i);
      SB();
    }
    if constexpr (j > JQ + 2) { pv(j - 8); SB(); }
    if constexpr (j == JS) {
      lds_wait(&L.mcnt, (unsigned)NWAVE);
      const float mm = L.ml[2 * c], m8 = max8(mm), M = fmaxf(m8, dpp<ROR8>(m8));
      wt = __builtin_amdgcn_exp2f((m_w - M) * LOG2E);
      asm volatile("" : "+v"(wt));
      STAMP(11);
      SB();
    }
  };
  step(IC<0>{});  step(IC<1>{});  step(IC<2>{});  step(IC<3>{});
  step(IC<4>{});  step(IC<5>{});  step(IC<6>{});  step(IC<7>{});
  step(IC<8>{});  step(IC<9>{});  step(IC<10>{});  step(IC<11>{});
  step(IC<12>{});  step(IC<13>{});  step(IC<14>{});  step(IC<15>{});
#pragma unroll
  for (int i = 16 + WND; i < 19; ++i) issue(i);
  {
    const f32x2 u = fold8(acc) * wt;
    *(f32x2*)(L.acc + w * 128 + 8 * c + 2 * r) = u;
  }
  STAMP(4);
  if (w == 0) {
    __syncthreads();
    SB();
    f32x2 a[NWAVE];
#pragma unroll
    for (int t = 0; t < NWAVE; ++t) a[t] = *(const f32x2*)(L.acc + t * 128 + 2 * lane);
#pragma unroll
    for (int st = 1; st < NWAVE; st *= 2)
#pragma unroll
      for (int t = 0; t < NWAVE; t += 2 * st) a[t] += a[t + st];
    const float o0 = a[0].x, o1 = a[0].y;
    gu64* og = ws + WS_OG + (h * NSPLIT + s) * DH;
    put(og + lane, o0, tag32, tag8);
    put(og + 64 + lane, o1, tag32, tag8);
    STAMP(5);
    PPT(5);
    int idx[17];
#pragma unroll
    for (int t = 0; t < 8; ++t) { idx[2 * t] = WS_OG + (h * NSPLIT + t) * DH + lane; idx[2 * t + 1] = idx[2 * t] + 64; }
    idx[16] = WS_SG + h * 16 + c;
    float v[17];
    gather<17>(ws, idx, tag32, tag8, v);
    STAMP(10);
    const float so = dpp<ROR8>(v[16]);
    const float Ms = c < 8 ? v[16] : so, Lsp = c < 8 ? so : v[16];
    const float MM = max8(Ms);
    const float fs = __builtin_amdgcn_exp2f((Ms - MM) * LOG2E);
    const float ws8 = fs / sum8(fs * Lsp);
    float a0 = 0.f, a1 = 0.f;
#pragma unroll
    for (int t = 0; t < 8; ++t) {
      const float wtt = __builtin_bit_cast(float, __builtin_amdgcn_readlane(__builtin_bit_cast(int, ws8), t));
      a0 = fmaf(wtt, v[2 * t], a0); a1 = fmaf(wtt, v[2 * t + 1], a1);
    }
    *(f32x2*)(L.o + 2 * lane) = f32x2{rbf(a0), rbf(a1)};
  } else {
    __syncthreads();
    STAMP(5);
    if (w == 1) {
      float hv = 0.f;
      if (ffn) {
        int idx[16];
#pragma unroll
        for (int t = 0; t < 8; ++t) { idx[2 * t] = WS_FG + (h * NSPLIT + t) * 128 + lane; idx[2 * t + 1] = idx[2 * t] + 64; }
        float v[16];
        gather<16>(ws, idx, tag32, tag8, v);
        float g = 0.f, u = 0.f;
#pragma unroll
        for (int t = 0; t < 8; ++t) { g += v[2 * t]; u += v[2 * t + 1]; }
        g = rbf(g); u = rbf(u);
        const float sg = rbf(1.f / (1.f + __builtin_amdgcn_exp2f(-g * LOG2E)));
        hv = rbf(sg * u);
      }
      L.hv[lane] = hv;
      STAMP(9);
    }
  }
  __syncthreads();
  {
    const float coef[3] = {L.o[rho], L.o[rho + 64], L.hv[rho]};
    float pa[8];
#pragma unroll
    for (int k = 0; k < 8; ++k) pa[k] = 0.f;
#pragma unroll
    for (int i = 0; i < 3; ++i) {
      const unsigned chi = __float_as_uint(coef[i]), clo = chi >> 16;
#pragma unroll
      for (int k = 0; k < 4; ++k) {
        pa[2 * k] = dot2(ld[16 + i][k], clo, pa[2 * k]);
        pa[2 * k + 1] = dot2(ld[16 + i][k], chi, pa[2 * k + 1]);
      }
    }
    const f32x2 u = fold8(pa);
    *(f32x2*)(L.p + w * 128 + 8 * c + 2 * r) = u;
  }
  __syncthreads();
  STAMP(6);
  PPT(6);
  if (tid < 2) __hip_atomic_store(ws + WS_TM + b * 2 + tid, ((unsigned long long)tag32 << 32) | (unsigned)(tid ? pt1 : pt0), RLX_AGENT);
  if (tid < 80) {
    float a = 0.f;
#pragma unroll
    for (int t = 0; t < NWAVE; ++t) a += L.p[t * 128 + tid];
    put(ws + WS_PG + ((s * 5 + (tid >> 4)) * 32 + h) * 16 + (tid & 15), a, tag32, tag8);
  }
  if (w == 2 && h < 5) {
    int idx[8];
#pragma unroll
    for (int t = 0; t < 8; ++t) idx[t] = WS_PG + ((s * 5 + h) * 32 + 8 * r + t) * 16 + c;
    const int col = 80 * s + 16 * h + c;
    const float xv = bf_lo(((gcu16*)x_)[col]);
    float v[8];
    gather<8>(ws, idx, tag32, tag8, v);
    float a = 0.f;
#pragma unroll
    for (int t = 0; t < 8; ++t) a += v[t];
    a = xrow_sum(a);
    if (lane < 16) ((GAS unsigned short*)out_)[col] = (unsigned short)(cvtpk(xv + a, 0.f) & 0xffffu);
    if (b == 0) {
      unsigned long long tend;
      asm volatile("s_memrealtime %0\n\ts_waitcnt lgkmcnt(0)" : "=s"(tend) :: "memory");
      int mn = 0x7fffffff, mx = -0x7fffffff;
      for (unsigned spins = 0; spins < 100000; ++spins) {
        bool ok = true; mn = 0x7fffffff; mx = -0x7fffffff;
        for (int k = 0; k < 4; ++k) {
          const unsigned long long g0 = __hip_atomic_load(ws + WS_TM + (k * 64 + lane) * 2, RLX_AGENT), g1 = __hip_atomic_load(ws + WS_TM + (k * 64 + lane) * 2 + 1, RLX_AGENT);
          ok &= (unsigned)(g0 >> 32) == tag32 && (unsigned)(g1 >> 32) == tag32;
          mn = min(mn, (int)((unsigned)g0 - (unsigned)tend)); mx = max(mx, (int)((unsigned)g1 - (unsigned)tend));
        }
        if (__all(ok)) break;
      }
      for (int o = 32; o >= 1; o >>= 1) { mn = min(mn, __shfl_xor(mn, o)); mx = max(mx, __shfl_xor(mx, o)); }
      int add = PPOINT == 7 ? -mn : (mx - mn);
      add = __builtin_amdgcn_readfirstlane(add) * PMULT;
      if (add < 0) add = 0;
      if (add > 20000) add = 20000;
      unsigned long long t;
      do { asm volatile("s_memrealtime %0\n\ts_waitcnt lgkmcnt(0)" : "=s"(t) :: "memory"); } while (t < tend + 300 + add);
    }
    if (b == 0 && lane == 0) __hip_atomic_store(ws + WS_EPOCH, (((unsigned long long)L.tag[3] << 32) | L.tag[2]) + 1ull, RLX_AGENT);
  }
  asm volatile(".Lkcode_end:" ::: "memory");
}

extern "C" void kernel_launch(void* const* d_in, const int* in_sizes, int n_in, void* d_out, int out_size, void* d_ws,
                              size_t ws_size, hipStream_t stream) {
  (void)in_sizes; (void)n_in; (void)out_size; (void)ws_size;
  hipLaunchKernelGGL(fused_decode, dim3(256), dim3(NT), 0, stream, (const unsigned short*)d_in[0], (const unsigned short*)d_in[1],
                     (const unsigned short*)d_in[2], (const unsigned short*)d_in[3], (const unsigned short*)d_in[4],
                     (const unsigned short*)d_in[6], (const unsigned short*)d_in[7], (const unsigned short*)d_in[8],
                     (unsigned short*)d_out, (unsigned long long*)d_ws);
}
```

```cpp
#include <hip/hip_runtime.h>
#include <hip/hip_bf16.h>
#include <stdint.h>

#define GAS __attribute__((address_space(1)))
#define LAS __attribute__((address_space(3)))
typedef unsigned v4u __attribute__((ext_vector_type(4)));
typedef unsigned v2u __attribute__((ext_vector_type(2)));
typedef float f32x2 __attribute__((ext_vector_type(2)));
typedef float f32x4 __attribute__((ext_vector_type(4)));
typedef __attribute__((ext_vector_type(2))) __bf16 bf16x2;
typedef GAS unsigned gu32;
typedef GAS unsigned long long gu64;
typedef GAS const unsigned short gcu16;
typedef GAS const unsigned gcu32;
typedef GAS const v4u gcv4u;
#define RLX_AGENT __ATOMIC_RELAXED, __HIP_MEMORY_SCOPE_AGENT
#define RLX_WG __ATOMIC_RELAXED, __HIP_MEMORY_SCOPE_WORKGROUP

constexpr int D = 640, HQ = 32, DH = 128, SK = 32768, WIN = 4096, F = 1728;
constexpr int NSPLIT = 8, KPW = WIN / NSPLIT;
#define WSOFF 0
constexpr int NT = 1024, NWAVE = 16;
constexpr int NFG = F / 64;
constexpr unsigned SPIN_CAP = 1u << 16;

constexpr unsigned WS_QG = 1024;
constexpr unsigned WS_FG = WS_QG + HQ * NSPLIT * 1024;
constexpr unsigned WS_OG = WS_FG + HQ * NSPLIT * 1024;
constexpr unsigned WS_SG = WS_OG + HQ * NSPLIT * 1024;
constexpr unsigned WS_PG = WS_SG + HQ * 128;
constexpr unsigned WS_END = WS_PG + 8 * 5 * 32 * 128;

__device__ __forceinline__ float bf_lo(unsigned w) { return __builtin_bit_cast(float, w << 16); }
__device__ __forceinline__ float bf_hi(unsigned w) { return __builtin_bit_cast(float, w & 0xffff0000u); }
__device__ __forceinline__ unsigned cvtpk(float lo, float hi) {
  unsigned r;
  asm("v_cvt_pk_bf16_f32 %0, %1, %2" : "=v"(r) : "v"(lo), "v"(hi));
  return r;
}
__device__ __forceinline__ float rbf(float x) { return bf_lo(cvtpk(x, x)); }
__device__ __forceinline__ float dot2(unsigned a, unsigned b, float acc) {
  return __builtin_amdgcn_fdot2_f32_bf16(__builtin_bit_cast(bf16x2, a), __builtin_bit_cast(bf16x2, b), acc, false);
}
template <int CTRL> __device__ __forceinline__ float dpp(float x) {
  return __builtin_bit_cast(float, __builtin_amdgcn_mov_dpp(__builtin_bit_cast(int, x), CTRL, 0xf, 0xf, true));
}
constexpr int XOR1 = 0xB1, XOR2 = 0x4E, HMIR = 0x141, ROR8 = 0x128;
__device__ __forceinline__ float sum8(float x) { x += dpp<XOR1>(x); x += dpp<XOR2>(x); x += dpp<HMIR>(x); return x; }
__device__ __forceinline__ float max8(float x) { x = fmaxf(x, dpp<XOR1>(x)); x = fmaxf(x, dpp<XOR2>(x)); x = fmaxf(x, dpp<HMIR>(x)); return x; }
__device__ __forceinline__ float row_sum(float x) { x = sum8(x); x += dpp<ROR8>(x); return x; }
__device__ __forceinline__ float fold32(float a, float b) {
  auto t = __builtin_amdgcn_permlane32_swap(__float_as_uint(a), __float_as_uint(b), false, false);
  return __uint_as_float(t[0]) + __uint_as_float(t[1]);
}
__device__ __forceinline__ float fold16(float a, float b) {
  auto t = __builtin_amdgcn_permlane16_swap(__float_as_uint(a), __float_as_uint(b), false, false);
  return __uint_as_float(t[0]) + __uint_as_float(t[1]);
}
__device__ __forceinline__ float xrow_sum(float x) { x = fold16(x, x); return fold32(x, x); }
__device__ __forceinline__ float xrow_max(float x) {
  auto s = __builtin_amdgcn_permlane16_swap(__float_as_uint(x), __float_as_uint(x), false, false);
  x = fmaxf(__uint_as_float(s[0]), __uint_as_float(s[1]));
  auto t = __builtin_amdgcn_permlane32_swap(__float_as_uint(x), __float_as_uint(x), false, false);
  return fmaxf(__uint_as_float(t[0]), __uint_as_float(t[1]));
}
__device__ __forceinline__ float wave_sum(float x) { return xrow_sum(row_sum(x)); }
__device__ __forceinline__ f32x2 fold8(const float (&v)[8]) {
  float t0 = fold32(v[0], v[4]), t1 = fold32(v[1], v[5]), t2 = fold32(v[2], v[6]), t3 = fold32(v[3], v[7]);
  f32x2 u;
  u.x = fold16(t0, t2);
  u.y = fold16(t1, t3);
  return u;
}

__device__ __forceinline__ v4u ld_nt(gcv4u* p) { return __builtin_nontemporal_load(p); }
typedef __amdgpu_buffer_rsrc_t rsrc_t;
__device__ __forceinline__ rsrc_t mk(const void* p, unsigned bytes) { return __builtin_amdgcn_make_buffer_rsrc((void*)p, 0, bytes, 0x00020000); }
template <int AUX> __device__ __forceinline__ v4u bld(rsrc_t r, unsigned voff, unsigned soff) { return __builtin_amdgcn_raw_buffer_load_b128(r, voff, soff, AUX); }
#define SB() __builtin_amdgcn_sched_barrier(0)
#define QST 18
#define QLD 18
#define FST 18
#define FLD 18
#define SST 18
#define OST 16
#define OLD 16
#define PST 16
#define PLD 16
#define PRE 0
#define SCAUX 16
constexpr int SC1 = SCAUX;
template <int AUX>
__device__ __forceinline__ void put2(rsrc_t rw, unsigned voff, unsigned soff, float a, float b, unsigned tag32, unsigned tag8) {
  const v4u g = {(__float_as_uint(a) & 0xffffff00u) | tag8, tag32, (__float_as_uint(b) & 0xffffff00u) | tag8, tag32};
  __builtin_amdgcn_raw_buffer_store_b128(g, rw, voff, soff, AUX);
}
template <int N, int AUX>
__device__ __forceinline__ void gather2(rsrc_t rw, unsigned voff, unsigned soff, unsigned stride, unsigned vlast, unsigned slast, unsigned tag32, unsigned tag8,
                                        f32x2 (&v)[N]) {
#pragma nounroll
  for (unsigned spins = 0;; ++spins) {
    v4u x[N];
#pragma unroll
    for (int k = 0; k < N; ++k) x[k] = k == N - 1 ? bld<AUX>(rw, vlast, slast) : bld<AUX>(rw, voff, soff + k * stride);
    unsigned bad = 0;
#pragma unroll
    for (int k = 0; k < N; ++k) {
      bad |= (x[k].y ^ tag32) | (x[k].w ^ tag32) | (((x[k].x ^ tag8) | (x[k].z ^ tag8)) & 0xffu);
      v[k] = f32x2{__uint_as_float(x[k].x & 0xffffff00u), __uint_as_float(x[k].z & 0xffffff00u)};
    }
    if (__all(bad == 0u)) return;
    if (spins > SPIN_CAP) {
#pragma unroll
      for (int k = 0; k < N; ++k) v[k] = f32x2{__uint_as_float(0x7fc00000u), __uint_as_float(0x7fc00000u)};
      return;
    }
    __builtin_amdgcn_s_sleep(1);
  }
}
#define STAMP(k)
constexpr int NTA = 2;

template <int N> struct IC { static constexpr int value = N; };
struct Lds {
  unsigned q[64];
  unsigned xn[40];
  unsigned tag[4];
  unsigned xflag, qflag, mcnt, gcnt;
  float ml[NWAVE * 2];
  float acc[NWAVE * 128];
  float p[NWAVE * 128];
  float gu[10 * 128];
  float o[128];
  float hv[64];
};
template <int N> __device__ __forceinline__ void waitvm() { asm volatile("s_waitcnt vmcnt(%0)" ::"i"(N) : "memory"); }
template <int A, int B> __device__ __forceinline__ void waitvm2(int w) {
  asm volatile("s_cmp_eq_u32 %0, 0\n\ts_cbranch_scc1 1f\n\ts_waitcnt vmcnt(%2)\n1:\n\ts_waitcnt vmcnt(%1)" ::"s"(w), "i"(A), "i"(B) : "scc", "memory");
}
__device__ __forceinline__ void lds_wait(unsigned* word, unsigned want) {
  unsigned spins = 0;
  while (__builtin_amdgcn_readfirstlane(__hip_atomic_load((LAS unsigned*)word, RLX_WG)) != want && ++spins < (1u << 22)) __builtin_amdgcn_s_sleep(2);
  asm volatile("" ::: "memory");
}
#define WND 2
#define WND0 4
#define JX 1
#define JS 14
#define JG 4
#define JQ 10
#define NPRE 19
#define JW 99
#define WND2 3
#define JF 8
#define PRIO 0
constexpr int nissued(int j) { const int n = j + 1 + (j < JW ? WND : WND2); return n < NPRE ? n : NPRE; }
#define ROTM 7

__global__ void __launch_bounds__(NT, 4)
fused_decode(const unsigned short* __restrict__ x_, const unsigned short* __restrict__ kc_, const unsigned short* __restrict__ vc_,
             const unsigned short* __restrict__ wq_, const unsigned short* __restrict__ wo_, const unsigned short* __restrict__ wg_,
             const unsigned short* __restrict__ wu_, const unsigned short* __restrict__ wd_, unsigned short* __restrict__ out_,
             unsigned long long* ws_) {
  __shared__ __attribute__((aligned(16))) Lds L;
  constexpr float LOG2E = 1.4426950408889634f;
  const int b = blockIdx.x, xcd = b & 7, bi = b >> 3;
  const int h = (bi >> 3) * 8 + xcd, s = bi & 7;
  const int tid = threadIdx.x, lane = tid & 63, w = __builtin_amdgcn_readfirstlane(tid >> 6), r = lane >> 4, c = lane & 15;
  const int rr = lane >> 3;
  const bool ffn = h < NFG;
  gu64* ws = (gu64*)ws_;
  const rsrc_t rw = mk(ws_, WS_END);
  v4u cpf;
  {
    unsigned long long pc;
    unsigned clen;
    asm volatile("s_getpc_b64 %0\n.Lkcode_begin:\n\ts_mov_b32 %1, .Lkcode_end-.Lkcode_begin" : "=s"(pc), "=s"(clen));
    const rsrc_t rc = mk((const void*)pc, clen);
    cpf = bld<0>(rc, tid * 16, 0);
    SB();
  }
  asm volatile("" ::"s"(x_), "s"(kc_), "s"(vc_), "s"(wq_), "s"(wo_), "s"(wg_), "s"(wu_), "s"(wd_), "s"(out_), "s"(ws_));
  STAMP(0);
  const rsrc_t rk = mk(kc_ + ((size_t)h * SK + (SK - WIN) + s * KPW + 32 * w) * DH, 32 * DH * 2);
  const rsrc_t rv = mk(vc_ + ((size_t)h * SK + (SK - WIN) + s * KPW + 32 * w) * DH, 32 * DH * 2);
  const rsrc_t ro = mk(wo_ + (size_t)(h * DH) * D + 80 * s, DH * D * 2 - 160 * s);
  const rsrc_t rd = mk(wd_ + (size_t)((ffn ? h : 0) * 64) * D + 80 * s, ffn ? 64 * D * 2 - 160 * s : 0);
  const unsigned ooff = c < 10 ? r * (D * 2) + 16 * c : 0x40000000u;
  const int rho = 4 * w + r;
  const int rot = (int)(((unsigned)(b * 16 + w) * 2654435761u) >> 16) & ROTM;
  v4u ld[19];
  auto issue = [&](int i) __attribute__((always_inline)) {
    if (i < 8) ld[i] = bld<NTA>(rk, lane * 16, ((i + rot) & 7) * 1024);
    else if (i < 16) ld[i] = bld<NTA>(rv, lane * 16, ((i + rot) & 7) * 1024);
    else if (i == 16) ld[i] = bld<NTA>(ro, ooff, w * (4 * D * 2));
    else if (i == 17) ld[i] = bld<NTA>(ro, ooff, w * (4 * D * 2) + 64 * D * 2);
    else if (i == 18) ld[i] = bld<NTA>(rd, ooff, w * (4 * D * 2));
    SB();
  };
  v4u gv, uv;
  if (w == 0) {
    __builtin_amdgcn_s_setprio(3);
    const rsrc_t rx = mk(x_, D * 2);
    const v4u a4 = bld<0>(rx, 20 * lane, 0);
    const unsigned a1 = __builtin_amdgcn_raw_buffer_load_b32(rx, 20 * lane + 16, 0, 0);
    const v4u b4 = bld<0>(rx, 20 * rr, 160 * s);
    const unsigned b1 = __builtin_amdgcn_raw_buffer_load_b32(rx, 20 * rr + 16, 160 * s, 0);
    const unsigned long long ep = __hip_atomic_load(ws, RLX_AGENT);
    SB();
    constexpr int QS = HQ * DH * 2;
    const rsrc_t rq = mk(wq_ + (size_t)(80 * s) * (HQ * DH) + h * DH, 79 * QS + 256);
    const unsigned woff = rr * 10 * QS + 16 * (lane & 7);
    v4u wv[20];
#pragma unroll
    for (int i = 0; i < 20; ++i) { wv[i] = bld<NTA>(rq, woff + (i & 1) * 128, (i >> 1) * QS); SB(); }
    if (lane == 0) { L.xflag = 0u; L.qflag = 0u; L.mcnt = 0u; L.gcnt = 0u; }
    __syncthreads();
    SB();
    const unsigned e_lo = __builtin_amdgcn_readfirstlane((unsigned)ep), e_hi = __builtin_amdgcn_readfirstlane((unsigned)(ep >> 32));
    unsigned tag32 = (e_lo + 1u) * 0x9E3779B1u; tag32 ^= tag32 >> 15; tag32 *= 0x85EBCA77u; tag32 ^= tag32 >> 13; tag32 ^= 0x5bd1e995u;
    const unsigned tag8 = (((e_hi ^ (e_lo * 0xC2B2AE35u)) * 0x27D4EB2Fu) >> 24) ^ 0x5Au;
    float xs[10];
#pragma unroll
    for (int i = 0; i < 4; ++i) { xs[2 * i] = bf_lo(a4[i]); xs[2 * i + 1] = bf_hi(a4[i]); }
    xs[8] = bf_lo(a1); xs[9] = bf_hi(a1);
    float sum = 0.f;
#pragma unroll
    for (int i = 0; i < 10; ++i) sum += xs[i];
    const float mean = wave_sum(sum) * (1.f / D);
    float sq = 0.f;
#pragma unroll
    for (int i = 0; i < 10; ++i) { const float d = xs[i] - mean; sq = fmaf(d, d, sq); }
    const float var = wave_sum(sq) * (1.f / D);
    const float mean_b = rbf(mean);
    const float rden = 1.f / rbf(sqrtf(rbf(rbf(var) + 1e-5f)));
    const unsigned xb[5] = {b4[0], b4[1], b4[2], b4[3], b1};
    unsigned xp[5];
#pragma unroll
    for (int i = 0; i < 5; ++i) xp[i] = cvtpk(rbf(bf_lo(xb[i]) - mean_b) * rden, rbf(bf_hi(xb[i]) - mean_b) * rden);
    if ((lane & 7) == 0) {
#pragma unroll
      for (int i = 0; i < 5; ++i) L.xn[5 * rr + i] = xp[i];
    }
    if (lane == 0) { L.tag[0] = tag32; L.tag[1] = tag8; L.tag[2] = e_lo; L.tag[3] = e_hi; }
    asm volatile("s_waitcnt lgkmcnt(0)" ::: "memory");
    if (lane == 0) __hip_atomic_store((LAS unsigned*)&L.xflag, 1u, RLX_WG);
    float accA[8], accB[8];
#pragma unroll
    for (int k = 0; k < 8; ++k) { accA[k] = 0.f; accB[k] = 0.f; }
#pragma unroll
    for (int i = 0; i < 10; ++i) {
      const unsigned xlo = (i & 1) ? xp[i >> 1] >> 16 : xp[i >> 1] & 0xffffu, xhi = (i & 1) ? xp[i >> 1] & 0xffff0000u : xp[i >> 1] << 16;
#pragma unroll
      for (int k = 0; k < 4; ++k) {
        accA[2 * k] = dot2(wv[2 * i][k], xlo, accA[2 * k]);
        accA[2 * k + 1] = dot2(wv[2 * i][k], xhi, accA[2 * k + 1]);
        accB[2 * k] = dot2(wv[2 * i + 1][k], xlo, accB[2 * k]);
        accB[2 * k + 1] = dot2(wv[2 * i + 1][k], xhi, accB[2 * k + 1]);
      }
    }
    f32x2 ua = fold8(accA), ub = fold8(accB);
    ua.x += dpp<ROR8>(ua.x); ua.y += dpp<ROR8>(ua.y); ub.x += dpp<ROR8>(ub.x); ub.y += dpp<ROR8>(ub.y);
    const f32x2 u = c < 8 ? ua : ub;
    put2<QST>(rw, lane * 16, WS_QG + (h * NSPLIT + s) * 1024, u.x, u.y, tag32, tag8);
    STAMP(1);
  } else {
    __syncthreads();
    if (PRIO == 1) { if (w >= 12) __builtin_amdgcn_s_setprio(3); else if (w >= 8) __builtin_amdgcn_s_setprio(2); else if (w >= 4) __builtin_amdgcn_s_setprio(1); }
    if (PRIO == 2) { if (w >= 8) __builtin_amdgcn_s_setprio(1); }
  }
  {
    const unsigned gbytes = __builtin_amdgcn_readfirstlane((ffn && w >= 1 && w <= 10) ? 7 * F * 2 + 128 : 0);
    const size_t goff = (size_t)(80 * s + 8 * ((w + 15) & 15)) * F + 64 * (ffn ? h : 0);
    const bool gon = w >= 1 && w <= 10;
    const rsrc_t rg = mk(wg_ + (gon ? goff : 0), gbytes), ru = mk(wu_ + (gon ? goff : 0), gbytes);
    SB();
    gv = bld<NTA>(rg, rr * (F * 2) + 16 * (lane & 7), 0);
    uv = bld<NTA>(ru, rr * (F * 2) + 16 * (lane & 7), 0);
    SB();
  }
  v4u qp;
  unsigned tag32 = 0, tag8 = 0;
  float sc[8], p[8], acc[8], m_w = 0.f;
#pragma unroll
  for (int k = 0; k < 8; ++k) acc[k] = 0.f;
  auto qk = [&](int i) __attribute__((always_inline)) {
    constexpr float SCALE = 0.08838834764831845f;
    float d = 0.f;
#pragma unroll
    for (int k = 0; k < 4; ++k) d = dot2(ld[i][k], qp[k], d);
    sc[i] = rbf(rbf(row_sum(d)) * SCALE);
  };
  auto pv = [&](int i) __attribute__((always_inline)) {
#pragma unroll
    for (int k = 0; k < 4; ++k) {
      acc[2 * k] = fmaf(p[i], bf_lo(ld[8 + i][k]), acc[2 * k]);
      acc[2 * k + 1] = fmaf(p[i], bf_hi(ld[8 + i][k]), acc[2 * k + 1]);
    }
    asm volatile("" : "+v"(acc[0]), "+v"(acc[1]), "+v"(acc[2]), "+v"(acc[3]), "+v"(acc[4]), "+v"(acc[5]), "+v"(acc[6]), "+v"(acc[7]));
  };
#pragma unroll
  for (int i = 0; i < WND; ++i) issue(i);
  auto step = [&](auto J) __attribute__((always_inline)) {
    constexpr int j = decltype(J)::value;
    if constexpr (nissued(j) > nissued(j - 1)) {
      waitvm2<nissued(j - 1) - j - 1 + WND0 - WND, nissued(j - 1) - j - 1>(w);
#pragma unroll
      for (int i = nissued(j - 1); i < nissued(j); ++i) issue(i);
    }
    if constexpr (j == JX) if (w == 0) {
      const unsigned t32 = __builtin_amdgcn_readfirstlane(L.tag[0]), t8 = __builtin_amdgcn_readfirstlane(L.tag[1]);
      f32x2 v[8];
      gather2<8, QLD>(rw, lane * 16, WS_QG + h * NSPLIT * 1024, 1024, lane * 16, WS_QG + (h * NSPLIT + 7) * 1024, t32, t8, v);
      const f32x2 qs = ((v[0] + v[1]) + (v[2] + v[3])) + ((v[4] + v[5]) + (v[6] + v[7]));
      const float q0 = qs.x, q1 = qs.y;
      L.q[4 * c + r] = cvtpk(q0, q1);
      asm volatile("s_waitcnt lgkmcnt(0)" ::: "memory");
      if (lane == 0) __hip_atomic_store((LAS unsigned*)&L.qflag, 1u, RLX_WG);
      STAMP(8);
      SB();
    }
    if constexpr (j == JG) {
      asm volatile("" ::"v"(cpf));
      lds_wait(&L.xflag, 1u);
      tag32 = __builtin_amdgcn_readfirstlane(L.tag[0]); tag8 = __builtin_amdgcn_readfirstlane(L.tag[1]);
      if (ffn && w >= 1 && w <= 10) {
        const unsigned xw = L.xn[4 * (w - 1) + (rr >> 1)];
        const float xn = (rr & 1) ? bf_hi(xw) : bf_lo(xw);
        float g[8], u[8];
#pragma unroll
        for (int k = 0; k < 4; ++k) {
          g[2 * k] = xn * bf_lo(gv[k]); g[2 * k + 1] = xn * bf_hi(gv[k]);
          u[2 * k] = xn * bf_lo(uv[k]); u[2 * k + 1] = xn * bf_hi(uv[k]);
        }
        f32x2 gs = fold8(g), us = fold8(u);
        gs.x += dpp<ROR8>(gs.x); gs.y += dpp<ROR8>(gs.y); us.x += dpp<ROR8>(us.x); us.y += dpp<ROR8>(us.y);
        *(f32x2*)(L.gu + (w - 1) * 128 + 8 * c + 2 * r) = c < 8 ? gs : us;
        asm volatile("s_waitcnt lgkmcnt(0)" ::: "memory");
        if (lane == 0) __hip_atomic_fetch_add((LAS unsigned*)&L.gcnt, 1u, RLX_WG);
      }
      SB();
    }
    if constexpr (j == JF) if (w == 0 && ffn) {
      lds_wait(&L.gcnt, 10u);
      float gg = 0.f, uu = 0.f;
#pragma unroll
      for (int t = 0; t < 10; ++t) { gg += L.gu[t * 128 + lane]; uu += L.gu[t * 128 + 64 + lane]; }
      put2<FST>(rw, lane * 16, WS_FG + (h * NSPLIT + s) * 1024, gg, uu, tag32, tag8);
      SB();
    }
    if constexpr (j == JQ) {
      STAMP(2);
      lds_wait(&L.qflag, 1u);
      qp = *(const v4u*)(L.q + 4 * c);
#pragma unroll
      for (int i = 0; i < 4; ++i) qk(i);
      SB();
    }
    if constexpr (j == JQ + 1) {
#pragma unroll
      for (int i = 4; i < 8; ++i) qk(i);
      float m = sc[0];
#pragma unroll
      for (int i = 1; i < 8; ++i) m = fmaxf(m, sc[i]);
      m_w = xrow_max(m);
      float l = 0.f;
#pragma unroll
      for (int i = 0; i < 8; ++i) { p[i] = __builtin_amdgcn_exp2f((sc[i] - m_w) * LOG2E); l += p[i]; }
      const float l_w = xrow_sum(l);
      if (lane == 0) {
        *(f32x2*)(L.ml + 2 * w) = f32x2{m_w, l_w};
        asm volatile("s_waitcnt lgkmcnt(0)" ::: "memory");
        __hip_atomic_fetch_add((LAS unsigned*)&L.mcnt, 1u, RLX_WG);
      }
      STAMP(3);
      SB();
    }
    if constexpr (j == JQ + 2) {
#pragma unroll
      for (int i = 0; i <= j - 8; ++i) pv(i);
      SB();
    }
    if constexpr (j > JQ + 2) { pv(j - 8); SB(); }
  };
  step(IC<0>{});  step(IC<1>{});  step(IC<2>{});  step(IC<3>{});
  step(IC<4>{});  step(IC<5>{});  step(IC<6>{});  step(IC<7>{});
  step(IC<8>{});  step(IC<9>{});  step(IC<10>{});  step(IC<11>{});
  step(IC<12>{});  step(IC<13>{});  step(IC<14>{});  step(IC<15>{});
#pragma unroll
  for (int i = nissued(15); i < NPRE; ++i) issue(i);
  {
    const f32x2 u = fold8(acc);
    *(f32x2*)(L.acc + w * 128 + 8 * c + 2 * r) = u;
  }
  STAMP(4);
  if (w == 0) {
    lds_wait(&L.mcnt, (unsigned)NWAVE);
    const f32x2 ml = *(const f32x2*)(L.ml + 2 * c);
    const float m8 = max8(ml.x), M = fmaxf(m8, dpp<ROR8>(m8));
    const float fl = __builtin_amdgcn_exp2f((ml.x - M) * LOG2E);
    const float Ls = row_sum(fl * ml.y);
    if (lane == 0) put2<SST>(rw, 0, WS_SG + h * 128 + s * 16, M, Ls, tag32, tag8);
    float fw[NWAVE];
#pragma unroll
    for (int t = 0; t < NWAVE; ++t) fw[t] = __builtin_bit_cast(float, __builtin_amdgcn_readlane(__builtin_bit_cast(int, fl), t));
    STAMP(11);
    SB();
    __syncthreads();
    SB();
    f32x2 a[NWAVE];
#pragma unroll
    for (int t = 0; t < NWAVE; ++t) a[t] = *(const f32x2*)(L.acc + t * 128 + 2 * lane);
    f32x2 oa = a[0] * fw[0], ob = a[1] * fw[1];
#pragma unroll
    for (int t = 2; t < NWAVE; t += 2) { oa += a[t] * fw[t]; ob += a[t + 1] * fw[t + 1]; }
    const float o0 = oa.x + ob.x, o1 = oa.y + ob.y;
    put2<OST>(rw, lane * 16, WS_OG + (h * NSPLIT + s) * 1024, o0, o1, tag32, tag8);
    SB();
#pragma unroll
    for (int i = NPRE; i < 19; ++i) issue(i);
    STAMP(5);
    f32x2 v[9];
    gather2<9, OLD>(rw, lane * 16, WS_OG + h * NSPLIT * 1024, 1024, (lane & 7) * 16, WS_SG + h * 128, tag32, tag8, v);
    STAMP(10);
    const float MM = max8(v[8].x);
    const float fs = __builtin_amdgcn_exp2f((v[8].x - MM) * LOG2E);
    const float ws8 = fs / sum8(fs * v[8].y);
    float a0 = 0.f, a1 = 0.f;
#pragma unroll
    for (int t = 0; t < 8; ++t) {
      const float wtt = __builtin_bit_cast(float, __builtin_amdgcn_readlane(__builtin_bit_cast(int, ws8), t));
      a0 = fmaf(wtt, v[t].x, a0); a1 = fmaf(wtt, v[t].y, a1);
    }
    *(f32x2*)(L.o + 2 * lane) = f32x2{rbf(a0), rbf(a1)};
  } else {
    __syncthreads();
    SB();
#pragma unroll
    for (int i = NPRE; i < 19; ++i) issue(i);
    STAMP(5);
    if (w == 1) {
      float hv = 0.f;
      if (ffn) {
        f32x2 v[8];
        gather2<8, FLD>(rw, lane * 16, WS_FG + h * NSPLIT * 1024, 1024, lane * 16, WS_FG + (h * NSPLIT + 7) * 1024, tag32, tag8, v);
        const f32x2 gu = ((v[0] + v[1]) + (v[2] + v[3])) + ((v[4] + v[5]) + (v[6] + v[7]));
        float g = gu.x, u = gu.y;
        g = rbf(g); u = rbf(u);
        const float sg = rbf(1.f / (1.f + __builtin_amdgcn_exp2f(-g * LOG2E)));
        hv = rbf(sg * u);
      }
      L.hv[lane] = hv;
      STAMP(9);
    }
  }
  __syncthreads();
  {
    const float coef[3] = {L.o[rho], L.o[rho + 64], L.hv[rho]};
    float pa[8];
#pragma unroll
    for (int k = 0; k < 8; ++k) pa[k] = 0.f;
#pragma unroll
    for (int i = 0; i < 3; ++i) {
      const unsigned chi = __float_as_uint(coef[i]), clo = chi >> 16;
#pragma unroll
      for (int k = 0; k < 4; ++k) {
        pa[2 * k] = dot2(ld[16 + i][k], clo, pa[2 * k]);
        pa[2 * k + 1] = dot2(ld[16 + i][k], chi, pa[2 * k + 1]);
      }
    }
    const f32x2 u = fold8(pa);
    *(f32x2*)(L.p + w * 128 + 8 * c + 2 * r) = u;
  }
  __syncthreads();
  STAMP(6);
  if (tid < 40) {
    f32x2 a[NWAVE];
#pragma unroll
    for (int t = 0; t < NWAVE; ++t) a[t] = *(const f32x2*)(L.p + t * 128 + 2 * tid);
#pragma unroll
    for (int st = 1; st < NWAVE; st *= 2)
#pragma unroll
      for (int t = 0; t < NWAVE; t += 2 * st) a[t] += a[t + st];
    put2<PST>(rw, (tid >> 3) * 4096 + (tid & 7) * 16, WS_PG + s * 5 * 4096 + h * 128, a[0].x, a[0].y, tag32, tag8);
  }
  if (w == 2 && h < 5) {
    const int col = 80 * s + 16 * h + 2 * (lane & 7);
    const unsigned xv = *(gcu32*)((gcu16*)x_ + col);
    f32x2 v[4];
    gather2<4, PLD>(rw, lane * 16, WS_PG + (s * 5 + h) * 4096, 1024, lane * 16, WS_PG + (s * 5 + h) * 4096 + 3072, tag32, tag8, v);
    f32x2 a = (v[0] + v[1]) + (v[2] + v[3]);
    a.x += dpp<ROR8>(a.x); a.y += dpp<ROR8>(a.y);
    a.x = xrow_sum(a.x); a.y = xrow_sum(a.y);
    if (lane < 8) *(GAS unsigned*)((GAS unsigned short*)out_ + col) = cvtpk(bf_lo(xv) + a.x, bf_hi(xv) + a.y);
    if (b == 0 && lane == 0) __hip_atomic_store(ws, (((unsigned long long)L.tag[3] << 32) | L.tag[2]) + 1ull, RLX_AGENT);
  }
  asm volatile(".Lkcode_end:" ::: "memory");
}

extern "C" void kernel_launch(void* const* d_in, const int* in_sizes, int n_in, void* d_out, int out_size, void* d_ws,
                              size_t ws_size, hipStream_t stream) {
  (void)in_sizes; (void)n_in; (void)out_size; (void)ws_size;
  hipLaunchKernelGGL(fused_decode, dim3(256), dim3(NT), 0, stream, (const unsigned short*)d_in[0], (const unsigned short*)d_in[1],
                     (const unsigned short*)d_in[2], (const unsigned short*)d_in[3], (const unsigned short*)d_in[4],
                     (const unsigned short*)d_in[6], (const unsigned short*)d_in[7], (const unsigned short*)d_in[8],
                     (unsigned short*)d_out, (unsigned long long*)((char*)d_ws + WSOFF));
}
```

```cpp
#define NOCPF
#include <hip/hip_runtime.h>
#include <hip/hip_bf16.h>
#include <stdint.h>

#define GAS __attribute__((address_space(1)))
#define LAS __attribute__((address_space(3)))
typedef unsigned v4u __attribute__((ext_vector_type(4)));
typedef unsigned v2u __attribute__((ext_vector_type(2)));
typedef float f32x2 __attribute__((ext_vector_type(2)));
typedef float f32x4 __attribute__((ext_vector_type(4)));
typedef __attribute__((ext_vector_type(2))) __bf16 bf16x2;
typedef GAS unsigned gu32;
typedef GAS unsigned long long gu64;
typedef GAS const unsigned short gcu16;
typedef GAS const unsigned gcu32;
typedef GAS const v4u gcv4u;
#define RLX_AGENT __ATOMIC_RELAXED, __HIP_MEMORY_SCOPE_AGENT
#define RLX_WG __ATOMIC_RELAXED, __HIP_MEMORY_SCOPE_WORKGROUP

constexpr int D = 640, HQ = 32, DH = 128, SK = 32768, WIN = 4096, F = 1728;
constexpr int NSPLIT = 8, KPW = WIN / NSPLIT;
#define WSOFF 0
constexpr int NT = 1024, NWAVE = 16;
constexpr int NFG = F / 64;
constexpr unsigned SPIN_CAP = 1u << 16;

constexpr unsigned WS_QG = 1024;
constexpr unsigned WS_FG = WS_QG + HQ * NSPLIT * 1024;
constexpr unsigned WS_OG = WS_FG + HQ * NSPLIT * 1024;
constexpr unsigned WS_SG = WS_OG + HQ * NSPLIT * 1024;
constexpr unsigned WS_PG = WS_SG + HQ * 128;
constexpr unsigned WS_END = WS_PG + 8 * 5 * 32 * 128;

__device__ __forceinline__ float bf_lo(unsigned w) { return __builtin_bit_cast(float, w << 16); }
__device__ __forceinline__ float bf_hi(unsigned w) { return __builtin_bit_cast(float, w & 0xffff0000u); }
__device__ __forceinline__ unsigned cvtpk(float lo, float hi) {
  unsigned r;
  asm("v_cvt_pk_bf16_f32 %0, %1, %2" : "=v"(r) : "v"(lo), "v"(hi));
  return r;
}
__device__ __forceinline__ float rbf(float x) { return bf_lo(cvtpk(x, x)); }
__device__ __forceinline__ float dot2(unsigned a, unsigned b, float acc) {
  return __builtin_amdgcn_fdot2_f32_bf16(__builtin_bit_cast(bf16x2, a), __builtin_bit_cast(bf16x2, b), acc, false);
}
template <int CTRL> __device__ __forceinline__ float dpp(float x) {
  return __builtin_bit_cast(float, __builtin_amdgcn_mov_dpp(__builtin_bit_cast(int, x), CTRL, 0xf, 0xf, true));
}
constexpr int XOR1 = 0xB1, XOR2 = 0x4E, HMIR = 0x141, ROR8 = 0x128;
__device__ __forceinline__ float sum8(float x) { x += dpp<XOR1>(x); x += dpp<XOR2>(x); x += dpp<HMIR>(x); return x; }
__device__ __forceinline__ float max8(float x) { x = fmaxf(x, dpp<XOR1>(x)); x = fmaxf(x, dpp<XOR2>(x)); x = fmaxf(x, dpp<HMIR>(x)); return x; }
__device__ __forceinline__ float row_sum(float x) { x = sum8(x); x += dpp<ROR8>(x); return x; }
__device__ __forceinline__ float fold32(float a, float b) {
  auto t = __builtin_amdgcn_permlane32_swap(__float_as_uint(a), __float_as_uint(b), false, false);
  return __uint_as_float(t[0]) + __uint_as_float(t[1]);
}
__device__ __forceinline__ float fold16(float a, float b) {
  auto t = __builtin_amdgcn_permlane16_swap(__float_as_uint(a), __float_as_uint(b), false, false);
  return __uint_as_float(t[0]) + __uint_as_float(t[1]);
}
__device__ __forceinline__ float xrow_sum(float x) { x = fold16(x, x); return fold32(x, x); }
__device__ __forceinline__ float xrow_max(float x) {
  auto s = __builtin_amdgcn_permlane16_swap(__float_as_uint(x), __float_as_uint(x), false, false);
  x = fmaxf(__uint_as_float(s[0]), __uint_as_float(s[1]));
  auto t = __builtin_amdgcn_permlane32_swap(__float_as_uint(x), __float_as_uint(x), false, false);
  return fmaxf(__uint_as_float(t[0]), __uint_as_float(t[1]));
}
__device__ __forceinline__ float wave_sum(float x) { return xrow_sum(row_sum(x)); }
__device__ __forceinline__ f32x2 fold8(const float (&v)[8]) {
  float t0 = fold32(v[0], v[4]), t1 = fold32(v[1], v[5]), t2 = fold32(v[2], v[6]), t3 = fold32(v[3], v[7]);
  f32x2 u;
  u.x = fold16(t0, t2);
  u.y = fold16(t1, t3);
  return u;
}

__device__ __forceinline__ v4u ld_nt(gcv4u* p) { return __builtin_nontemporal_load(p); }
typedef __amdgpu_buffer_rsrc_t rsrc_t;
__device__ __forceinline__ rsrc_t mk(const void* p, unsigned bytes) { return __builtin_amdgcn_make_buffer_rsrc((void*)p, 0, bytes, 0x00020000); }
template <int AUX> __device__ __forceinline__ v4u bld(rsrc_t r, unsigned voff, unsigned soff) { return __builtin_amdgcn_raw_buffer_load_b128(r, voff, soff, AUX); }
#define SB() __builtin_amdgcn_sched_barrier(0)
#define QST 18
#define QLD 18
#define FST 18
#define FLD 18
#define SST 18
#define OST 16
#define OLD 16
#define PST 16
#define PLD 16
#define PRE 0
#define SCAUX 16
constexpr int SC1 = SCAUX;
template <int AUX>
__device__ __forceinline__ void put2(rsrc_t rw, unsigned voff, unsigned soff, float a, float b, unsigned tag32, unsigned tag8) {
  const v4u g = {(__float_as_uint(a) & 0xffffff00u) | tag8, tag32, (__float_as_uint(b) & 0xffffff00u) | tag8, tag32};
  __builtin_amdgcn_raw_buffer_store_b128(g, rw, voff, soff, AUX);
}
template <int N, int AUX>
__device__ __forceinline__ void gather2(rsrc_t rw, unsigned voff, unsigned soff, unsigned stride, unsigned vlast, unsigned slast, unsigned tag32, unsigned tag8,
                                        f32x2 (&v)[N]) {
#pragma nounroll
  for (unsigned spins = 0;; ++spins) {
    v4u x[N];
#pragma unroll
    for (int k = 0; k < N; ++k) x[k] = k == N - 1 ? bld<AUX>(rw, vlast, slast) : bld<AUX>(rw, voff, soff + k * stride);
    unsigned bad = 0;
#pragma unroll
    for (int k = 0; k < N; ++k) {
      bad |= (x[k].y ^ tag32) | (x[k].w ^ tag32) | (((x[k].x ^ tag8) | (x[k].z ^ tag8)) & 0xffu);
      v[k] = f32x2{__uint_as_float(x[k].x & 0xffffff00u), __uint_as_float(x[k].z & 0xffffff00u)};
    }
    if (__all(bad == 0u)) return;
    if (spins > SPIN_CAP) {
#pragma unroll
      for (int k = 0; k < N; ++k) v[k] = f32x2{__uint_as_float(0x7fc00000u), __uint_as_float(0x7fc00000u)};
      return;
    }
    __builtin_amdgcn_s_sleep(1);
  }
}
#define STAMP(k)
constexpr int NTA = 2;
#define KAUX 2
#define VAUX 2
#define WAUX 2

template <int N> struct IC { static constexpr int value = N; };
struct Lds {
  unsigned q[64];
  unsigned xn[40];
  unsigned tag[4];
  unsigned xflag, qflag, mcnt, gcnt;
  float ml[NWAVE * 2];
  float acc[NWAVE * 128];
  float p[NWAVE * 128];
  float gu[10 * 128];
  float o[128];
  float hv[64];
};
template <int N> __device__ __forceinline__ void waitvm() { asm volatile("s_waitcnt vmcnt(%0)" ::"i"(N) : "memory"); }
template <int A, int B> __device__ __forceinline__ void waitvm2(int w) {
  asm volatile("s_cmp_eq_u32 %0, 0\n\ts_cbranch_scc1 1f\n\ts_waitcnt vmcnt(%2)\n1:\n\ts_waitcnt vmcnt(%1)" ::"s"(w), "i"(A), "i"(B) : "scc", "memory");
}
__device__ __forceinline__ void lds_wait(unsigned* word, unsigned want) {
  unsigned spins = 0;
  while (__builtin_amdgcn_readfirstlane(__hip_atomic_load((LAS unsigned*)word, RLX_WG)) != want && ++spins < (1u << 22)) __builtin_amdgcn_s_sleep(2);
  asm volatile("" ::: "memory");
}
#define WND 2
#define WND0 4
#define JX 1
#define JS 14
#define JG 4
#define JQ 10
#define NPRE 19
#define JW 99
#define WND2 3
#define JF 8
#define PRIO 0
constexpr int nissued(int j) { const int n = j + 1 + (j < JW ? WND : WND2); return n < NPRE ? n : NPRE; }
#define ROTM 7

__global__ void __launch_bounds__(NT, 4)
fused_decode(const unsigned short* __restrict__ x_, const unsigned short* __restrict__ kc_, const unsigned short* __restrict__ vc_,
             const unsigned short* __restrict__ wq_, const unsigned short* __restrict__ wo_, const unsigned short* __restrict__ wg_,
             const unsigned short* __restrict__ wu_, const unsigned short* __restrict__ wd_, unsigned short* __restrict__ out_,
             unsigned long long* ws_) {
  __shared__ __attribute__((aligned(16))) Lds L;
  constexpr float LOG2E = 1.4426950408889634f;
  const int b = blockIdx.x, xcd = b & 7, bi = b >> 3;
  const int h = (bi >> 3) * 8 + xcd, s = bi & 7;
  const int tid = threadIdx.x, lane = tid & 63, w = __builtin_amdgcn_readfirstlane(tid >> 6), r = lane >> 4, c = lane & 15;
  const int rr = lane >> 3;
  const bool ffn = h < NFG;
  gu64* ws = (gu64*)ws_;
  const rsrc_t rw = mk(ws_, WS_END);
  v4u cpf;
  {
    unsigned long long pc;
    unsigned clen;
    asm volatile("s_getpc_b64 %0\n.Lkcode_begin:\n\ts_mov_b32 %1, .Lkcode_end-.Lkcode_begin" : "=s"(pc), "=s"(clen));
    const rsrc_t rc = mk((const void*)pc, clen);
    cpf = bld<0>(rc, tid * 16 + 0x100000, 0);
    SB();
  }
  asm volatile("" ::"s"(x_), "s"(kc_), "s"(vc_), "s"(wq_), "s"(wo_), "s"(wg_), "s"(wu_), "s"(wd_), "s"(out_), "s"(ws_));
  STAMP(0);
  const rsrc_t rk = mk(kc_ + ((size_t)h * SK + (SK - WIN) + s * KPW + 32 * w) * DH, 32 * DH * 2);
  const rsrc_t rv = mk(vc_ + ((size_t)h * SK + (SK - WIN) + s * KPW + 32 * w) * DH, 32 * DH * 2);
  const rsrc_t ro = mk(wo_ + (size_t)(h * DH) * D + 80 * s, DH * D * 2 - 160 * s);
  const rsrc_t rd = mk(wd_ + (size_t)((ffn ? h : 0) * 64) * D + 80 * s, ffn ? 64 * D * 2 - 160 * s : 0);
  const unsigned ooff = c < 10 ? r * (D * 2) + 16 * c : 0x40000000u;
  const int rho = 4 * w + r;
  const int rot = (int)(((unsigned)(b * 16 + w) * 2654435761u) >> 16) & ROTM;
  v4u ld[19];
  auto issue = [&](int i) __attribute__((always_inline)) {
    if (i < 8) ld[i] = bld<KAUX>(rk, lane * 16, ((i + rot) & 7) * 1024);
    else if (i < 16) ld[i] = bld<VAUX>(rv, lane * 16, ((i + rot) & 7) * 1024);
    else if (i == 16) ld[i] = bld<WAUX>(ro, ooff, w * (4 * D * 2));
    else if (i == 17) ld[i] = bld<WAUX>(ro, ooff, w * (4 * D * 2) + 64 * D * 2);
    else if (i == 18) ld[i] = bld<WAUX>(rd, ooff, w * (4 * D * 2));
    SB();
  };
  v4u gv, uv;
  if (w == 0) {
    __builtin_amdgcn_s_setprio(3);
    const rsrc_t rx = mk(x_, D * 2);
    const v4u a4 = bld<0>(rx, 20 * lane, 0);
    const unsigned a1 = __builtin_amdgcn_raw_buffer_load_b32(rx, 20 * lane + 16, 0, 0);
    const v4u b4 = bld<0>(rx, 20 * rr, 160 * s);
    const unsigned b1 = __builtin_amdgcn_raw_buffer_load_b32(rx, 20 * rr + 16, 160 * s, 0);
    const unsigned long long ep = __hip_atomic_load(ws, RLX_AGENT);
    SB();
    constexpr int QS = HQ * DH * 2;
    const rsrc_t rq = mk(wq_ + (size_t)(80 * s) * (HQ * DH) + h * DH, 79 * QS + 256);
    const unsigned woff = rr * 10 * QS + 16 * (lane & 7);
    v4u wv[20];
#pragma unroll
    for (int i = 0; i < 20; ++i) { wv[i] = bld<WAUX>(rq, woff + (i & 1) * 128, (i >> 1) * QS); SB(); }
    if (lane == 0) { L.xflag = 0u; L.qflag = 0u; L.mcnt = 0u; L.gcnt = 0u; }
    __syncthreads();
    SB();
    const unsigned e_lo = __builtin_amdgcn_readfirstlane((unsigned)ep), e_hi = __builtin_amdgcn_readfirstlane((unsigned)(ep >> 32));
    unsigned tag32 = (e_lo + 1u) * 0x9E3779B1u; tag32 ^= tag32 >> 15; tag32 *= 0x85EBCA77u; tag32 ^= tag32 >> 13; tag32 ^= 0x5bd1e995u;
    const unsigned tag8 = (((e_hi ^ (e_lo * 0xC2B2AE35u)) * 0x27D4EB2Fu) >> 24) ^ 0x5Au;
    float xs[10];
#pragma unroll
    for (int i = 0; i < 4; ++i) { xs[2 * i] = bf_lo(a4[i]); xs[2 * i + 1] = bf_hi(a4[i]); }
    xs[8] = bf_lo(a1); xs[9] = bf_hi(a1);
    float sum = 0.f;
#pragma unroll
    for (int i = 0; i < 10; ++i) sum += xs[i];
    const float mean = wave_sum(sum) * (1.f / D);
    float sq = 0.f;
#pragma unroll
    for (int i = 0; i < 10; ++i) { const float d = xs[i] - mean; sq = fmaf(d, d, sq); }
    const float var = wave_sum(sq) * (1.f / D);
    const float mean_b = rbf(mean);
    const float rden = 1.f / rbf(sqrtf(rbf(rbf(var) + 1e-5f)));
    const unsigned xb[5] = {b4[0], b4[1], b4[2], b4[3], b1};
    unsigned xp[5];
#pragma unroll
    for (int i = 0; i < 5; ++i) xp[i] = cvtpk(rbf(bf_lo(xb[i]) - mean_b) * rden, rbf(bf_hi(xb[i]) - mean_b) * rden);
    if ((lane & 7) == 0) {
#pragma unroll
      for (int i = 0; i < 5; ++i) L.xn[5 * rr + i] = xp[i];
    }
    if (lane == 0) { L.tag[0] = tag32; L.tag[1] = tag8; L.tag[2] = e_lo; L.tag[3] = e_hi; }
    asm volatile("s_waitcnt lgkmcnt(0)" ::: "memory");
    if (lane == 0) __hip_atomic_store((LAS unsigned*)&L.xflag, 1u, RLX_WG);
    float accA[8], accB[8];
#pragma unroll
    for (int k = 0; k < 8; ++k) { accA[k] = 0.f; accB[k] = 0.f; }
#pragma unroll
    for (int i = 0; i < 10; ++i) {
      const unsigned xlo = (i & 1) ? xp[i >> 1] >> 16 : xp[i >> 1] & 0xffffu, xhi = (i & 1) ? xp[i >> 1] & 0xffff0000u : xp[i >> 1] << 16;
#pragma unroll
      for (int k = 0; k < 4; ++k) {
        accA[2 * k] = dot2(wv[2 * i][k], xlo, accA[2 * k]);
        accA[2 * k + 1] = dot2(wv[2 * i][k], xhi, accA[2 * k + 1]);
        accB[2 * k] = dot2(wv[2 * i + 1][k], xlo, accB[2 * k]);
        accB[2 * k + 1] = dot2(wv[2 * i + 1][k], xhi, accB[2 * k + 1]);
      }
    }
    f32x2 ua = fold8(accA), ub = fold8(accB);
    ua.x += dpp<ROR8>(ua.x); ua.y += dpp<ROR8>(ua.y); ub.x += dpp<ROR8>(ub.x); ub.y += dpp<ROR8>(ub.y);
    const f32x2 u = c < 8 ? ua : ub;
    put2<QST>(rw, lane * 16, WS_QG + (h * NSPLIT + s) * 1024, u.x, u.y, tag32, tag8);
    STAMP(1);
  } else {
    __syncthreads();
    if (PRIO == 1) { if (w >= 12) __builtin_amdgcn_s_setprio(3); else if (w >= 8) __builtin_amdgcn_s_setprio(2); else if (w >= 4) __builtin_amdgcn_s_setprio(1); }
    if (PRIO == 2) { if (w >= 8) __builtin_amdgcn_s_setprio(1); }
  }
  {
    const unsigned gbytes = __builtin_amdgcn_readfirstlane((ffn && w >= 1 && w <= 10) ? 7 * F * 2 + 128 : 0);
    const size_t goff = (size_t)(80 * s + 8 * ((w + 15) & 15)) * F + 64 * (ffn ? h : 0);
    const bool gon = w >= 1 && w <= 10;
    const rsrc_t rg = mk(wg_ + (gon ? goff : 0), gbytes), ru = mk(wu_ + (gon ? goff : 0), gbytes);
    SB();
    gv = bld<WAUX>(rg, rr * (F * 2) + 16 * (lane & 7), 0);
    uv = bld<WAUX>(ru, rr * (F * 2) + 16 * (lane & 7), 0);
    SB();
  }
  v4u qp;
  unsigned tag32 = 0, tag8 = 0;
  float sc[8], p[8], acc[8], m_w = 0.f;
#pragma unroll
  for (int k = 0; k < 8; ++k) acc[k] = 0.f;
  auto qk = [&](int i) __attribute__((always_inline)) {
    constexpr float SCALE = 0.08838834764831845f;
    float d = 0.f;
#pragma unroll
    for (int k = 0; k < 4; ++k) d = dot2(ld[i][k], qp[k], d);
    sc[i] = rbf(rbf(row_sum(d)) * SCALE);
  };
  auto pv = [&](int i) __attribute__((always_inline)) {
#pragma unroll
    for (int k = 0; k < 4; ++k) {
      acc[2 * k] = fmaf(p[i], bf_lo(ld[8 + i][k]), acc[2 * k]);
      acc[2 * k + 1] = fmaf(p[i], bf_hi(ld[8 + i][k]), acc[2 * k + 1]);
    }
    asm volatile("" : "+v"(acc[0]), "+v"(acc[1]), "+v"(acc[2]), "+v"(acc[3]), "+v"(acc[4]), "+v"(acc[5]), "+v"(acc[6]), "+v"(acc[7]));
  };
#pragma unroll
  for (int i = 0; i < WND; ++i) issue(i);
  auto step = [&](auto J) __attribute__((always_inline)) {
    constexpr int j = decltype(J)::value;
    if constexpr (nissued(j) > nissued(j - 1)) {
      waitvm2<nissued(j - 1) - j - 1 + WND0 - WND, nissued(j - 1) - j - 1>(w);
#pragma unroll
      for (int i = nissued(j - 1); i < nissued(j); ++i) issue(i);
    }
    if constexpr (j == JX) if (w == 0) {
      const unsigned t32 = __builtin_amdgcn_readfirstlane(L.tag[0]), t8 = __builtin_amdgcn_readfirstlane(L.tag[1]);
      f32x2 v[8];
      gather2<8, QLD>(rw, lane * 16, WS_QG + h * NSPLIT * 1024, 1024, lane * 16, WS_QG + (h * NSPLIT + 7) * 1024, t32, t8, v);
      const f32x2 qs = ((v[0] + v[1]) + (v[2] + v[3])) + ((v[4] + v[5]) + (v[6] + v[7]));
      const float q0 = qs.x, q1 = qs.y;
      L.q[4 * c + r] = cvtpk(q0, q1);
      asm volatile("s_waitcnt lgkmcnt(0)" ::: "memory");
      if (lane == 0) __hip_atomic_store((LAS unsigned*)&L.qflag, 1u, RLX_WG);
      STAMP(8);
      SB();
    }
    if constexpr (j == JG) {
      asm volatile("" ::"v"(cpf));
      lds_wait(&L.xflag, 1u);
      tag32 = __builtin_amdgcn_readfirstlane(L.tag[0]); tag8 = __builtin_amdgcn_readfirstlane(L.tag[1]);
      if (ffn && w >= 1 && w <= 10) {
        const unsigned xw = L.xn[4 * (w - 1) + (rr >> 1)];
        const float xn = (rr & 1) ? bf_hi(xw) : bf_lo(xw);
        float g[8], u[8];
#pragma unroll
        for (int k = 0; k < 4; ++k) {
          g[2 * k] = xn * bf_lo(gv[k]); g[2 * k + 1] = xn * bf_hi(gv[k]);
          u[2 * k] = xn * bf_lo(uv[k]); u[2 * k + 1] = xn * bf_hi(uv[k]);
        }
        f32x2 gs = fold8(g), us = fold8(u);
        gs.x += dpp<ROR8>(gs.x); gs.y += dpp<ROR8>(gs.y); us.x += dpp<ROR8>(us.x); us.y += dpp<ROR8>(us.y);
        *(f32x2*)(L.gu + (w - 1) * 128 + 8 * c + 2 * r) = c < 8 ? gs : us;
        asm volatile("s_waitcnt lgkmcnt(0)" ::: "memory");
        if (lane == 0) __hip_atomic_fetch_add((LAS unsigned*)&L.gcnt, 1u, RLX_WG);
      }
      SB();
    }
    if constexpr (j == JF) if (w == 0 && ffn) {
      lds_wait(&L.gcnt, 10u);
      float gg = 0.f, uu = 0.f;
#pragma unroll
      for (int t = 0; t < 10; ++t) { gg += L.gu[t * 128 + lane]; uu += L.gu[t * 128 + 64 + lane]; }
      put2<FST>(rw, lane * 16, WS_FG + (h * NSPLIT + s) * 1024, gg, uu, tag32, tag8);
      SB();
    }
    if constexpr (j == JQ) {
      STAMP(2);
      lds_wait(&L.qflag, 1u);
      qp = *(const v4u*)(L.q + 4 * c);
#pragma unroll
      for (int i = 0; i < 4; ++i) qk(i);
      SB();
    }
    if constexpr (j == JQ + 1) {
#pragma unroll
      for (int i = 4; i < 8; ++i) qk(i);
      float m = sc[0];
#pragma unroll
      for (int i = 1; i < 8; ++i) m = fmaxf(m, sc[i]);
      m_w = xrow_max(m);
      float l = 0.f;
#pragma unroll
      for (int i = 0; i < 8; ++i) { p[i] = __builtin_amdgcn_exp2f((sc[i] - m_w) * LOG2E); l += p[i]; }
      const float l_w = xrow_sum(l);
      if (lane == 0) {
        *(f32x2*)(L.ml + 2 * w) = f32x2{m_w, l_w};
        asm volatile("s_waitcnt lgkmcnt(0)" ::: "memory");
        __hip_atomic_fetch_add((LAS unsigned*)&L.mcnt, 1u, RLX_WG);
      }
      STAMP(3);
      SB();
    }
    if constexpr (j == JQ + 2) {
#pragma unroll
      for (int i = 0; i <= j - 8; ++i) pv(i);
      SB();
    }
    if constexpr (j > JQ + 2) { pv(j - 8); SB(); }
  };
  step(IC<0>{});  step(IC<1>{});  step(IC<2>{});  step(IC<3>{});
  step(IC<4>{});  step(IC<5>{});  step(IC<6>{});  step(IC<7>{});
  step(IC<8>{});  step(IC<9>{});  step(IC<10>{});  step(IC<11>{});
  step(IC<12>{});  step(IC<13>{});  step(IC<14>{});  step(IC<15>{});
#pragma unroll
  for (int i = nissued(15); i < NPRE; ++i) issue(i);
  {
    const f32x2 u = fold8(acc);
    *(f32x2*)(L.acc + w * 128 + 8 * c + 2 * r) = u;
  }
  STAMP(4);
  if (w == 0) {
    lds_wait(&L.mcnt, (unsigned)NWAVE);
    const f32x2 ml = *(const f32x2*)(L.ml + 2 * c);
    const float m8 = max8(ml.x), M = fmaxf(m8, dpp<ROR8>(m8));
    const float fl = __builtin_amdgcn_exp2f((ml.x - M) * LOG2E);
    const float Ls = row_sum(fl * ml.y);
    if (lane == 0) put2<SST>(rw, 0, WS_SG + h * 128 + s * 16, M, Ls, tag32, tag8);
    float fw[NWAVE];
#pragma unroll
    for (int t = 0; t < NWAVE; ++t) fw[t] = __builtin_bit_cast(float, __builtin_amdgcn_readlane(__builtin_bit_cast(int, fl), t));
    STAMP(11);
    SB();
    __syncthreads();
    SB();
    f32x2 a[NWAVE];
#pragma unroll
    for (int t = 0; t < NWAVE; ++t) a[t] = *(const f32x2*)(L.acc + t * 128 + 2 * lane);
    f32x2 oa = a[0] * fw[0], ob = a[1] * fw[1];
#pragma unroll
    for (int t = 2; t < NWAVE; t += 2) { oa += a[t] * fw[t]; ob += a[t + 1] * fw[t + 1]; }
    const float o0 = oa.x + ob.x, o1 = oa.y + ob.y;
    put2<OST>(rw, lane * 16, WS_OG + (h * NSPLIT + s) * 1024, o0, o1, tag32, tag8);
    SB();
#pragma unroll
    for (int i = NPRE; i < 19; ++i) issue(i);
    STAMP(5);
    f32x2 v[9];
    gather2<9, OLD>(rw, lane * 16, WS_OG + h * NSPLIT * 1024, 1024, (lane & 7) * 16, WS_SG + h * 128, tag32, tag8, v);
    STAMP(10);
    const float MM = max8(v[8].x);
    const float fs = __builtin_amdgcn_exp2f((v[8].x - MM) * LOG2E);
    const float ws8 = fs / sum8(fs * v[8].y);
    float a0 = 0.f, a1 = 0.f;
#pragma unroll
    for (int t = 0; t < 8; ++t) {
      const float wtt = __builtin_bit_cast(float, __builtin_amdgcn_readlane(__builtin_bit_cast(int, ws8), t));
      a0 = fmaf(wtt, v[t].x, a0); a1 = fmaf(wtt, v[t].y, a1);
    }
    *(f32x2*)(L.o + 2 * lane) = f32x2{rbf(a0), rbf(a1)};
  } else {
    __syncthreads();
    SB();
#pragma unroll
    for (int i = NPRE; i < 19; ++i) issue(i);
    STAMP(5);
    if (w == 1) {
      float hv = 0.f;
      if (ffn) {
        f32x2 v[8];
        gather2<8, FLD>(rw, lane * 16, WS_FG + h * NSPLIT * 1024, 1024, lane * 16, WS_FG + (h * NSPLIT + 7) * 1024, tag32, tag8, v);
        const f32x2 gu = ((v[0] + v[1]) + (v[2] + v[3])) + ((v[4] + v[5]) + (v[6] + v[7]));
        float g = gu.x, u = gu.y;
        g = rbf(g); u = rbf(u);
        const float sg = rbf(1.f / (1.f + __builtin_amdgcn_exp2f(-g * LOG2E)));
        hv = rbf(sg * u);
      }
      L.hv[lane] = hv;
      STAMP(9);
    }
  }
  __syncthreads();
  {
    const float coef[3] = {L.o[rho], L.o[rho + 64], L.hv[rho]};
    float pa[8];
#pragma unroll
    for (int k = 0; k < 8; ++k) pa[k] = 0.f;
#pragma unroll
    for (int i = 0; i < 3; ++i) {
      const unsigned chi = __float_as_uint(coef[i]), clo = chi >> 16;
#pragma unroll
      for (int k = 0; k < 4; ++k) {
        pa[2 * k] = dot2(ld[16 + i][k], clo, pa[2 * k]);
        pa[2 * k + 1] = dot2(ld[16 + i][k], chi, pa[2 * k + 1]);
      }
    }
    const f32x2 u = fold8(pa);
    *(f32x2*)(L.p + w * 128 + 8 * c + 2 * r) = u;
  }
  __syncthreads();
  STAMP(6);
  if (tid < 40) {
    f32x2 a[NWAVE];
#pragma unroll
    for (int t = 0; t < NWAVE; ++t) a[t] = *(const f32x2*)(L.p + t * 128 + 2 * tid);
#pragma unroll
    for (int st = 1; st < NWAVE; st *= 2)
#pragma unroll
      for (int t = 0; t < NWAVE; t += 2 * st) a[t] += a[t + st];
    put2<PST>(rw, (tid >> 3) * 4096 + (tid & 7) * 16, WS_PG + s * 5 * 4096 + h * 128, a[0].x, a[0].y, tag32, tag8);
  }
  if (w == 2 && h < 5) {
    const int col = 80 * s + 16 * h + 2 * (lane & 7);
    const unsigned xv = *(gcu32*)((gcu16*)x_ + col);
    f32x2 v[4];
    gather2<4, PLD>(rw, lane * 16, WS_PG + (s * 5 + h) * 4096, 1024, lane * 16, WS_PG + (s * 5 + h) * 4096 + 3072, tag32, tag8, v);
    f32x2 a = (v[0] + v[1]) + (v[2] + v[3]);
    a.x += dpp<ROR8>(a.x); a.y += dpp<ROR8>(a.y);
    a.x = xrow_sum(a.x); a.y = xrow_sum(a.y);
    if (lane < 8) *(GAS unsigned*)((GAS unsigned short*)out_ + col) = cvtpk(bf_lo(xv) + a.x, bf_hi(xv) + a.y);
    if (b == 0 && lane == 0) __hip_atomic_store(ws, (((unsigned long long)L.tag[3] << 32) | L.tag[2]) + 1ull, RLX_AGENT);
  }
  asm volatile(".Lkcode_end:" ::: "memory");
}

extern "C" void kernel_launch(void* const* d_in, const int* in_sizes, int n_in, void* d_out, int out_size, void* d_ws,
                              size_t ws_size, hipStream_t stream) {
  (void)in_sizes; (void)n_in; (void)out_size; (void)ws_size;
  hipLaunchKernelGGL(fused_decode, dim3(256), dim3(NT), 0, stream, (const unsigned short*)d_in[0], (const unsigned short*)d_in[1],
                     (const unsigned short*)d_in[2], (const unsigned short*)d_in[3], (const unsigned short*)d_in[4],
                     (const unsigned short*)d_in[6], (const unsigned short*)d_in[7], (const unsigned short*)d_in[8],
                     (unsigned short*)d_out, (unsigned long long*)((char*)d_ws + WSOFF));
}
```

```cpp
#include <hip/hip_runtime.h>
#include <hip/hip_bf16.h>
#include <stdint.h>

#define GAS __attribute__((address_space(1)))
#define LAS __attribute__((address_space(3)))
typedef unsigned v4u __attribute__((ext_vector_type(4)));
typedef unsigned v2u __attribute__((ext_vector_type(2)));
typedef float f32x2 __attribute__((ext_vector_type(2)));
typedef float f32x4 __attribute__((ext_vector_type(4)));
typedef __attribute__((ext_vector_type(2))) __bf16 bf16x2;
typedef __attribute__((ext_vector_type(8))) __bf16 bf16x8;
typedef GAS unsigned gu32;
typedef GAS unsigned long long gu64;
typedef GAS const unsigned short gcu16;
typedef GAS const unsigned gcu32;
typedef GAS const v4u gcv4u;
#define RLX_AGENT __ATOMIC_RELAXED, __HIP_MEMORY_SCOPE_AGENT
#define RLX_WG __ATOMIC_RELAXED, __HIP_MEMORY_SCOPE_WORKGROUP

constexpr int D = 640, HQ = 32, DH = 128, SK = 32768, WIN = 4096, F = 1728;
constexpr int NSPLIT = 8, KPW = WIN / NSPLIT;
#define WSOFF 0
constexpr int NT = 1024, NWAVE = 16;
constexpr int NFG = F / 64;
constexpr unsigned SPIN_CAP = 1u << 16;

constexpr unsigned WS_QG = 1024;
constexpr unsigned WS_FG = WS_QG + HQ * NSPLIT * 1024;
constexpr unsigned WS_OG = WS_FG + HQ * NSPLIT * 1024;
constexpr unsigned WS_SG = WS_OG + HQ * NSPLIT * 1024;
constexpr unsigned WS_PG = WS_SG + HQ * 128;
constexpr unsigned WS_END = WS_PG + 8 * 5 * 32 * 128;

__device__ __forceinline__ float bf_lo(unsigned w) { return __builtin_bit_cast(float, w << 16); }
__device__ __forceinline__ float bf_hi(unsigned w) { return __builtin_bit_cast(float, w & 0xffff0000u); }
__device__ __forceinline__ unsigned cvtpk(float lo, float hi) {
  unsigned r;
  asm("v_cvt_pk_bf16_f32 %0, %1, %2" : "=v"(r) : "v"(lo), "v"(hi));
  return r;
}
__device__ __forceinline__ float rbf(float x) { return bf_lo(cvtpk(x, x)); }
__device__ __forceinline__ float dot2(unsigned a, unsigned b, float acc) {
  return __builtin_amdgcn_fdot2_f32_bf16(__builtin_bit_cast(bf16x2, a), __builtin_bit_cast(bf16x2, b), acc, false);
}
__device__ __forceinline__ f32x4 mfma_rows(unsigned pk, const v4u& sel, const v4u& wreg, f32x4 acc) {
  const v4u a = {pk & sel.x, pk & sel.y, pk & sel.z, pk & sel.w};
  return __builtin_amdgcn_mfma_f32_16x16x32_bf16(__builtin_bit_cast(bf16x8, a), __builtin_bit_cast(bf16x8, wreg), acc, 0, 0, 0);
}
template <int CTRL> __device__ __forceinline__ float dpp(float x) {
  return __builtin_bit_cast(float, __builtin_amdgcn_mov_dpp(__builtin_bit_cast(int, x), CTRL, 0xf, 0xf, true));
}
constexpr int XOR1 = 0xB1, XOR2 = 0x4E, HMIR = 0x141, ROR8 = 0x128;
__device__ __forceinline__ float sum8(float x) { x += dpp<XOR1>(x); x += dpp<XOR2>(x); x += dpp<HMIR>(x); return x; }
__device__ __forceinline__ float max8(float x) { x = fmaxf(x, dpp<XOR1>(x)); x = fmaxf(x, dpp<XOR2>(x)); x = fmaxf(x, dpp<HMIR>(x)); return x; }
__device__ __forceinline__ float row_sum(float x) { x = sum8(x); x += dpp<ROR8>(x); return x; }
__device__ __forceinline__ float fold32(float a, float b) {
  auto t = __builtin_amdgcn_permlane32_swap(__float_as_uint(a), __float_as_uint(b), false, false);
  return __uint_as_float(t[0]) + __uint_as_float(t[1]);
}
__device__ __forceinline__ float fold16(float a, float b) {
  auto t = __builtin_amdgcn_permlane16_swap(__float_as_uint(a), __float_as_uint(b), false, false);
  return __uint_as_float(t[0]) + __uint_as_float(t[1]);
}
__device__ __forceinline__ float xrow_sum(float x) { x = fold16(x, x); return fold32(x, x); }
__device__ __forceinline__ float xrow_max(float x) {
  auto s = __builtin_amdgcn_permlane16_swap(__float_as_uint(x), __float_as_uint(x), false, false);
  x = fmaxf(__uint_as_float(s[0]), __uint_as_float(s[1]));
  auto t = __builtin_amdgcn_permlane32_swap(__float_as_uint(x), __float_as_uint(x), false, false);
  return fmaxf(__uint_as_float(t[0]), __uint_as_float(t[1]));
}
__device__ __forceinline__ float wave_sum(float x) { return xrow_sum(row_sum(x)); }
__device__ __forceinline__ f32x2 fold8(const float (&v)[8]) {
  float t0 = fold32(v[0], v[4]), t1 = fold32(v[1], v[5]), t2 = fold32(v[2], v[6]), t3 = fold32(v[3], v[7]);
  f32x2 u;
  u.x = fold16(t0, t2);
  u.y = fold16(t1, t3);
  return u;
}

__device__ __forceinline__ v4u ld_nt(gcv4u* p) { return __builtin_nontemporal_load(p); }
typedef __amdgpu_buffer_rsrc_t rsrc_t;
__device__ __forceinline__ rsrc_t mk(const void* p, unsigned bytes) { return __builtin_amdgcn_make_buffer_rsrc((void*)p, 0, bytes, 0x00020000); }
template <int AUX> __device__ __forceinline__ v4u bld(rsrc_t r, unsigned voff, unsigned soff) { return __builtin_amdgcn_raw_buffer_load_b128(r, voff, soff, AUX); }
#define SB() __builtin_amdgcn_sched_barrier(0)
#define QST 16
#define QLD 16
#define FST 16
#define FLD 16
#define SST 16
#define OST 16
#define OLD 16
#define PST 16
#define PLD 16
#define PRE 0
#define SCAUX 16
constexpr int SC1 = SCAUX;
template <int AUX>
__device__ __forceinline__ void put2(rsrc_t rw, unsigned voff, unsigned soff, float a, float b, unsigned tag32, unsigned tag8) {
  const v4u g = {(__float_as_uint(a) & 0xffffff00u) | tag8, tag32, (__float_as_uint(b) & 0xffffff00u) | tag8, tag32};
  __builtin_amdgcn_raw_buffer_store_b128(g, rw, voff, soff, AUX);
}
struct TagChk {
  unsigned b32 = 0, b8 = 0;
  __device__ __forceinline__ f32x2 take(const v4u& x, unsigned tag32, unsigned tag8) {
    b32 |= __builtin_amdgcn_bitop3_b32(x.y, x.w, tag32, 0x7e);
    const unsigned a = x.x ^ tag8, b = x.z ^ tag8;
    b8 |= a | b;
    return f32x2{__uint_as_float(a), __uint_as_float(b)};
  }
  __device__ __forceinline__ bool ok() const { return __all((b32 | (b8 & 0xffu)) == 0u); }
};
template <int N, int AUX>
__device__ __forceinline__ f32x2 gather_sum(rsrc_t rw, unsigned voff, unsigned soff, unsigned stride, unsigned tag32, unsigned tag8) {
  f32x2 sum;
#pragma nounroll
  for (unsigned spins = 0; spins < SPIN_CAP; ++spins) {
    v4u x[N];
#pragma unroll
    for (int k = 0; k < N; ++k) x[k] = bld<AUX>(rw, voff, soff + k * stride);
    SB();
    TagChk tc;
    f32x2 v[N];
#pragma unroll
    for (int k = 0; k < N; ++k) v[k] = tc.take(x[k], tag32, tag8);
    sum = v[0];
#pragma unroll
    for (int k = 1; k < N; ++k) sum += v[k];
    asm volatile("" : "+v"(sum));
    if (tc.ok()) return sum;
    __builtin_amdgcn_s_sleep(1);
  }
  return f32x2{__uint_as_float(0x7fc00000u), __uint_as_float(0x7fc00000u)};
}
#define STAMP(k)
constexpr int NTA = 2;
#define KAUX 2
#define VAUX 2
#define WAUX 2

template <int N> struct IC { static constexpr int value = N; };
struct Lds {
  unsigned q[64];
  unsigned xn[40];
  unsigned tag[4];
  unsigned xflag, qflag, mcnt, gcnt;
  float ml[NWAVE * 2];
  float acc[NWAVE * 128];
  float p[NWAVE * 128];
  float gu[10 * 128];
  unsigned o[128];
  unsigned hv[64];
};
template <int N> __device__ __forceinline__ void waitvm() { asm volatile("s_waitcnt vmcnt(%0)" ::"i"(N) : "memory"); }
template <int A, int B> __device__ __forceinline__ void waitvm2(int w) {
  asm volatile("s_cmp_eq_u32 %0, 0\n\ts_cbranch_scc1 1f\n\ts_waitcnt vmcnt(%2)\n1:\n\ts_waitcnt vmcnt(%1)" ::"s"(w), "i"(A), "i"(B) : "scc", "memory");
}
__device__ __forceinline__ void lds_wait(unsigned* word, unsigned want) {
  unsigned spins = 0;
  while (__builtin_amdgcn_readfirstlane(__hip_atomic_load((LAS unsigned*)word, RLX_WG)) != want && ++spins < (1u << 22)) __builtin_amdgcn_s_sleep(2);
  asm volatile("" ::: "memory");
}
#define WND 2
#define WND0 4
#define JX 1
#define JS 14
#define JG 4
#define JQ 10
#define NPRE 19
#define JW 99
#define WND2 3
#define JH 12
#define HW 0
#define JF 6
#define PRIO 0
constexpr int nissued(int j) { const int n = j + 1 + (j < JW ? WND : WND2); return n < NPRE ? n : NPRE; }
#define ROTM 7

__global__ void __launch_bounds__(NT, 4)
fused_decode(const unsigned short* __restrict__ x_, const unsigned short* __restrict__ kc_, const unsigned short* __restrict__ vc_,
             const unsigned short* __restrict__ wq_, const unsigned short* __restrict__ wo_, const unsigned short* __restrict__ wg_,
             const unsigned short* __restrict__ wu_, const unsigned short* __restrict__ wd_, unsigned short* __restrict__ out_,
             unsigned long long* ws_) {
  __shared__ __attribute__((aligned(16))) Lds L;
  constexpr float LOG2E = 1.4426950408889634f;
  const int b = blockIdx.x, xcd = b & 7, bi = b >> 3;
  const int h = (bi >> 3) * 8 + xcd, s = bi & 7;
  const int tid = threadIdx.x, lane = tid & 63, w = __builtin_amdgcn_readfirstlane(tid >> 6), r = lane >> 4, c = lane & 15;
  const int rr = lane >> 3;
  const bool ffn = h < NFG;
  gu64* ws = (gu64*)ws_;
  const rsrc_t rw = mk(ws_, WS_END);
  v4u cpf;
  {
    unsigned long long pc;
    unsigned clen;
    asm volatile("s_getpc_b64 %0\n.Lkcode_begin:\n\ts_mov_b32 %1, .Lkcode_end-.Lkcode_begin" : "=s"(pc), "=s"(clen));
    const rsrc_t rc = mk((const void*)pc, clen);
    cpf = bld<0>(rc, tid * 16, 0);
    SB();
  }
  asm volatile("" ::"s"(x_), "s"(kc_), "s"(vc_), "s"(wq_), "s"(wo_), "s"(wg_), "s"(wu_), "s"(wd_), "s"(out_), "s"(ws_));
  STAMP(0);
  const rsrc_t rk = mk(kc_ + ((size_t)h * SK + (SK - WIN) + s * KPW + 32 * w) * DH, 32 * DH * 2);
  const rsrc_t rv = mk(vc_ + ((size_t)h * SK + (SK - WIN) + s * KPW + 32 * w) * DH, 32 * DH * 2);
  const rsrc_t ro = mk(wo_ + (size_t)(h * DH) * D + 80 * s, DH * D * 2 - 160 * s);
  const rsrc_t rd = mk(wd_ + (size_t)((ffn ? h : 0) * 64) * D + 80 * s, ffn ? 64 * D * 2 - 160 * s : 0);
  const unsigned ooff = c < 10 ? r * (D * 2) + 16 * c : 0x40000000u;
  const unsigned ooff2 = c < 10 ? r * (2 * D * 2) + 16 * c : 0x40000000u;
  v4u sel;
  {
    const unsigned hm = c < 8 ? ((c & 1) ? 0xffff0000u : 0x0000ffffu) : 0u;
    sel = v4u{(c >> 1) == 0 ? hm : 0u, (c >> 1) == 1 ? hm : 0u, (c >> 1) == 2 ? hm : 0u, (c >> 1) == 3 ? hm : 0u};
  }
  const int rho = 4 * w + r;
  const int rot = (int)(((unsigned)(b * 16 + w) * 2654435761u) >> 16) & ROTM;
  v4u ld[19];
  auto issue = [&](int i) __attribute__((always_inline)) {
    if (i < 8) ld[i] = bld<KAUX>(rk, lane * 16, ((i + rot) & 7) * 1024);
    else if (i < 16) ld[i] = bld<VAUX>(rv, lane * 16, ((i + rot) & 7) * 1024);
    else if (i == 16) ld[i] = bld<WAUX>(ro, ooff2, w * (8 * D * 2));
    else if (i == 17) ld[i] = bld<WAUX>(ro, ooff2, w * (8 * D * 2) + D * 2);
    else if (i == 18) ld[i] = bld<WAUX>(rd, ooff, w * (4 * D * 2));
    SB();
  };
  v4u gv, uv;
  if (w == 0) {
    __builtin_amdgcn_s_setprio(3);
    const rsrc_t rx = mk(x_, D * 2);
    const v4u a4 = bld<0>(rx, 20 * lane, 0);
    const unsigned a1 = __builtin_amdgcn_raw_buffer_load_b32(rx, 20 * lane + 16, 0, 0);
    const v4u b4 = bld<0>(rx, 20 * rr, 160 * s);
    const unsigned b1 = __builtin_amdgcn_raw_buffer_load_b32(rx, 20 * rr + 16, 160 * s, 0);
    const unsigned long long ep = __hip_atomic_load(ws, RLX_AGENT);
    SB();
    constexpr int QS = HQ * DH * 2;
    const rsrc_t rq = mk(wq_ + (size_t)(80 * s) * (HQ * DH) + h * DH, 79 * QS + 256);
    const unsigned woff = rr * 10 * QS + 16 * (lane & 7);
    v4u wv[20];
#pragma unroll
    for (int i = 0; i < 20; ++i) { wv[i] = bld<WAUX>(rq, woff + (i & 1) * 128, (i >> 1) * QS); SB(); }
    if (lane == 0) { L.xflag = 0u; L.qflag = 0u; L.mcnt = 0u; L.gcnt = 0u; }
    __syncthreads();
    SB();
    const unsigned e_lo = __builtin_amdgcn_readfirstlane((unsigned)ep), e_hi = __builtin_amdgcn_readfirstlane((unsigned)(ep >> 32));
    unsigned tag32 = (e_lo + 1u) * 0x9E3779B1u; tag32 ^= tag32 >> 15; tag32 *= 0x85EBCA77u; tag32 ^= tag32 >> 13; tag32 ^= 0x5bd1e995u;
    const unsigned tag8 = (((e_hi ^ (e_lo * 0xC2B2AE35u)) * 0x27D4EB2Fu) >> 24) ^ 0x5Au;
    float xs[10];
#pragma unroll
    for (int i = 0; i < 4; ++i) { xs[2 * i] = bf_lo(a4[i]); xs[2 * i + 1] = bf_hi(a4[i]); }
    xs[8] = bf_lo(a1); xs[9] = bf_hi(a1);
    float sum = 0.f;
#pragma unroll
    for (int i = 0; i < 10; ++i) sum += xs[i];
    const float mean = wave_sum(sum) * (1.f / D);
    float sq = 0.f;
#pragma unroll
    for (int i = 0; i < 10; ++i) { const float d = xs[i] - mean; sq = fmaf(d, d, sq); }
    const float var = wave_sum(sq) * (1.f / D);
    const float mean_b = rbf(mean);
    const float rden = 1.f / rbf(sqrtf(rbf(rbf(var) + 1e-5f)));
    const unsigned xb[5] = {b4[0], b4[1], b4[2], b4[3], b1};
    unsigned xp[5];
#pragma unroll
    for (int i = 0; i < 5; ++i) xp[i] = cvtpk(rbf(bf_lo(xb[i]) - mean_b) * rden, rbf(bf_hi(xb[i]) - mean_b) * rden);
    if ((lane & 7) == 0) {
#pragma unroll
      for (int i = 0; i < 5; ++i) L.xn[5 * rr + i] = xp[i];
    }
    if (lane == 0) { L.tag[0] = tag32; L.tag[1] = tag8; L.tag[2] = e_lo; L.tag[3] = e_hi; }
    asm volatile("s_waitcnt lgkmcnt(0)" ::: "memory");
    if (lane == 0) __hip_atomic_store((LAS unsigned*)&L.xflag, 1u, RLX_WG);
    float accA[8], accB[8];
#pragma unroll
    for (int k = 0; k < 8; ++k) { accA[k] = 0.f; accB[k] = 0.f; }
#pragma unroll
    for (int i = 0; i < 10; ++i) {
      const unsigned xlo = (i & 1) ? xp[i >> 1] >> 16 : xp[i >> 1] & 0xffffu, xhi = (i & 1) ? xp[i >> 1] & 0xffff0000u : xp[i >> 1] << 16;
#pragma unroll
      for (int k = 0; k < 4; ++k) {
        accA[2 * k] = dot2(wv[2 * i][k], xlo, accA[2 * k]);
        accA[2 * k + 1] = dot2(wv[2 * i][k], xhi, accA[2 * k + 1]);
        accB[2 * k] = dot2(wv[2 * i + 1][k], xlo, accB[2 * k]);
        accB[2 * k + 1] = dot2(wv[2 * i + 1][k], xhi, accB[2 * k + 1]);
      }
    }
    f32x2 ua = fold8(accA), ub = fold8(accB);
    ua.x += dpp<ROR8>(ua.x); ua.y += dpp<ROR8>(ua.y); ub.x += dpp<ROR8>(ub.x); ub.y += dpp<ROR8>(ub.y);
    const f32x2 u = c < 8 ? ua : ub;
    put2<QST>(rw, lane * 16, WS_QG + (h * NSPLIT + s) * 1024, u.x, u.y, tag32, tag8);
    STAMP(1);
  } else {
    __syncthreads();
    if (PRIO == 1) { if (w >= 12) __builtin_amdgcn_s_setprio(3); else if (w >= 8) __builtin_amdgcn_s_setprio(2); else if (w >= 4) __builtin_amdgcn_s_setprio(1); }
    if (PRIO == 2) { if (w >= 8) __builtin_amdgcn_s_setprio(1); }
  }
  {
    const unsigned gbytes = __builtin_amdgcn_readfirstlane((ffn && w >= 1 && w <= 10) ? 7 * F * 2 + 128 : 0);
    const size_t goff = (size_t)(80 * s + 8 * ((w + 15) & 15)) * F + 64 * (ffn ? h : 0);
    const bool gon = w >= 1 && w <= 10;
    const rsrc_t rg = mk(wg_ + (gon ? goff : 0), gbytes), ru = mk(wu_ + (gon ? goff : 0), gbytes);
    SB();
    gv = bld<WAUX>(rg, rr * (F * 2) + 16 * (lane & 7), 0);
    uv = bld<WAUX>(ru, rr * (F * 2) + 16 * (lane & 7), 0);
    SB();
  }
  v4u qp;
  unsigned tag32 = 0, tag8 = 0;
  float sc[8], m_w = 0.f;
  unsigned pk[8];
  f32x4 acc = {0.f, 0.f, 0.f, 0.f};
  auto qk = [&](int i) __attribute__((always_inline)) {
    constexpr float SCALE = 0.08838834764831845f;
    float d = 0.f;
#pragma unroll
    for (int k = 0; k < 4; ++k) d = dot2(ld[i][k], qp[k], d);
    sc[i] = rbf(rbf(row_sum(d)) * SCALE);
  };
  auto pv = [&](int i) __attribute__((always_inline)) {
    acc = mfma_rows(pk[i], sel, ld[8 + i], acc);
    asm volatile("" : "+v"(acc));
  };
#pragma unroll
  for (int i = 0; i < WND; ++i) issue(i);
  auto step = [&](auto J) __attribute__((always_inline)) {
    constexpr int j = decltype(J)::value;
    if constexpr (nissued(j) > nissued(j - 1)) {
      waitvm2<nissued(j - 1) - j - 1 + WND0 - WND, nissued(j - 1) - j - 1>(w);
#pragma unroll
      for (int i = nissued(j - 1); i < nissued(j); ++i) issue(i);
    }
    if constexpr (j == JX) if (w == 0) {
      const unsigned t32 = __builtin_amdgcn_readfirstlane(L.tag[0]), t8 = __builtin_amdgcn_readfirstlane(L.tag[1]);
      const f32x2 qs = gather_sum<8, QLD>(rw, lane * 16, WS_QG + h * NSPLIT * 1024, 1024, t32, t8);
      const float q0 = qs.x, q1 = qs.y;
      L.q[4 * c + r] = cvtpk(q0, q1);
      asm volatile("s_waitcnt lgkmcnt(0)" ::: "memory");
      if (lane == 0) __hip_atomic_store((LAS unsigned*)&L.qflag, 1u, RLX_WG);
      STAMP(8);
      SB();
    }
    if constexpr (j == JG) {
      asm volatile("" ::"v"(cpf));
      lds_wait(&L.xflag, 1u);
      tag32 = __builtin_amdgcn_readfirstlane(L.tag[0]); tag8 = __builtin_amdgcn_readfirstlane(L.tag[1]);
      if (ffn && w >= 1 && w <= 10) {
        const unsigned xw = L.xn[4 * (w - 1) + (rr >> 1)];
        const float xn = (rr & 1) ? bf_hi(xw) : bf_lo(xw);
        float g[8], u[8];
#pragma unroll
        for (int k = 0; k < 4; ++k) {
          g[2 * k] = xn * bf_lo(gv[k]); g[2 * k + 1] = xn * bf_hi(gv[k]);
          u[2 * k] = xn * bf_lo(uv[k]); u[2 * k + 1] = xn * bf_hi(uv[k]);
        }
        f32x2 gs = fold8(g), us = fold8(u);
        gs.x += dpp<ROR8>(gs.x); gs.y += dpp<ROR8>(gs.y); us.x += dpp<ROR8>(us.x); us.y += dpp<ROR8>(us.y);
        *(f32x2*)(L.gu + (w - 1) * 128 + 8 * c + 2 * r) = c < 8 ? gs : us;
        asm volatile("s_waitcnt lgkmcnt(0)" ::: "memory");
        if (lane == 0) __hip_atomic_fetch_add((LAS unsigned*)&L.gcnt, 1u, RLX_WG);
      }
      SB();
    }
    if constexpr (j == JF) if (w == 0 && ffn) {
      lds_wait(&L.gcnt, 10u);
      float gg = 0.f, uu = 0.f;
#pragma unroll
      for (int t = 0; t < 10; ++t) { gg += L.gu[t * 128 + lane]; uu += L.gu[t * 128 + 64 + lane]; }
      put2<FST>(rw, lane * 16, WS_FG + (h * NSPLIT + s) * 1024, gg, uu, tag32, tag8);
      SB();
    }
    if constexpr (j == JQ) {
      STAMP(2);
      lds_wait(&L.qflag, 1u);
      qp = *(const v4u*)(L.q + 4 * c);
#pragma unroll
      for (int i = 0; i < 4; ++i) qk(i);
      SB();
    }
    if constexpr (j == JQ + 1) {
#pragma unroll
      for (int i = 4; i < 8; ++i) qk(i);
      float m = sc[0];
#pragma unroll
      for (int i = 1; i < 8; ++i) m = fmaxf(m, sc[i]);
      m_w = xrow_max(m);
      float l = 0.f;
#pragma unroll
      for (int i = 0; i < 8; ++i) { const float pe = __builtin_amdgcn_exp2f((sc[i] - m_w) * LOG2E); l += pe; pk[i] = cvtpk(pe, pe); }
      const float l_w = xrow_sum(l);
      if (lane == 0) {
        *(f32x2*)(L.ml + 2 * w) = f32x2{m_w, l_w};
        asm volatile("s_waitcnt lgkmcnt(0)" ::: "memory");
        __hip_atomic_fetch_add((LAS unsigned*)&L.mcnt, 1u, RLX_WG);
      }
      STAMP(3);
      SB();
    }
    if constexpr (j == JQ + 2) {
#pragma unroll
      for (int i = 0; i <= j - 8; ++i) pv(i);
      SB();
    }
    if constexpr (j > JQ + 2) { pv(j - 8); SB(); }
    if constexpr (j == JH) if (w == HW) {
      float hv = 0.f;
      if (ffn) {
        const f32x2 gu = gather_sum<8, FLD>(rw, lane * 16, WS_FG + h * NSPLIT * 1024, 1024, tag32, tag8);
        float g = gu.x, u = gu.y;
        g = rbf(g); u = rbf(u);
        const float sg = rbf(1.f / (1.f + __builtin_amdgcn_exp2f(-g * LOG2E)));
        hv = rbf(sg * u);
      }
      L.hv[lane] = cvtpk(hv, hv);
      STAMP(9);
    }
  };
  step(IC<0>{});  step(IC<1>{});  step(IC<2>{});  step(IC<3>{});
  step(IC<4>{});  step(IC<5>{});  step(IC<6>{});  step(IC<7>{});
  step(IC<8>{});  step(IC<9>{});  step(IC<10>{});  step(IC<11>{});
  step(IC<12>{});  step(IC<13>{});  step(IC<14>{});  step(IC<15>{});
#pragma unroll
  for (int i = nissued(15); i < NPRE; ++i) issue(i);
  if (lane < 32) *(f32x4*)(L.acc + w * 128 + 8 * c + 4 * r) = acc;
  STAMP(4);
  if (w == 0) {
    lds_wait(&L.mcnt, (unsigned)NWAVE);
    const f32x2 ml = *(const f32x2*)(L.ml + 2 * c);
    const float m8 = max8(ml.x), M = fmaxf(m8, dpp<ROR8>(m8));
    const float fl = __builtin_amdgcn_exp2f((ml.x - M) * LOG2E);
    const float Ls = row_sum(fl * ml.y);
    put2<SST>(rw, lane == 0 ? 0u : 0x08000000u, WS_SG + h * 128 + s * 16, M, Ls, tag32, tag8);
    float fw[NWAVE];
#pragma unroll
    for (int t = 0; t < NWAVE; ++t) fw[t] = __builtin_bit_cast(float, __builtin_amdgcn_readlane(__builtin_bit_cast(int, fl), t));
    STAMP(11);
    SB();
    __syncthreads();
    SB();
    f32x2 a[NWAVE];
#pragma unroll
    for (int t = 0; t < NWAVE; ++t) a[t] = *(const f32x2*)(L.acc + t * 128 + 2 * lane);
    f32x2 oa = a[0] * fw[0], ob = a[1] * fw[1];
#pragma unroll
    for (int t = 2; t < NWAVE; t += 2) { oa += a[t] * fw[t]; ob += a[t + 1] * fw[t + 1]; }
    const float o0 = oa.x + ob.x, o1 = oa.y + ob.y;
    put2<OST>(rw, lane * 16, WS_OG + (h * NSPLIT + s) * 1024, o0, o1, tag32, tag8);
    SB();
#pragma unroll
    for (int i = NPRE; i < 19; ++i) issue(i);
    STAMP(5);
    f32x2 o2 = {__uint_as_float(0x7fc00000u), __uint_as_float(0x7fc00000u)};
#pragma nounroll
    for (unsigned spins = 0; spins < SPIN_CAP; ++spins) {
      const v4u xs = bld<OLD>(rw, (lane & 7) * 16, WS_SG + h * 128);
      v4u x[8];
#pragma unroll
      for (int k = 0; k < 8; ++k) x[k] = bld<OLD>(rw, lane * 16, WS_OG + (h * NSPLIT + k) * 1024);
      SB();
      TagChk tc;
      const f32x2 st = tc.take(xs, tag32, tag8);
      const float MM = max8(st.x);
      const float fs = __builtin_amdgcn_exp2f((st.x - MM) * LOG2E);
      const float ws8 = fs * __builtin_amdgcn_rcpf(sum8(fs * st.y));
      float wk[8];
#pragma unroll
      for (int k = 0; k < 8; ++k) wk[k] = __builtin_bit_cast(float, __builtin_amdgcn_readlane(__builtin_bit_cast(int, ws8), k));
      SB();
      f32x2 acc2 = tc.take(x[0], tag32, tag8) * wk[0];
#pragma unroll
      for (int k = 1; k < 8; ++k) acc2 += tc.take(x[k], tag32, tag8) * wk[k];
      asm volatile("" : "+v"(acc2));
      if (tc.ok()) { o2 = acc2; break; }
      __builtin_amdgcn_s_sleep(1);
    }
    STAMP(10);
    *(v2u*)(L.o + 2 * lane) = v2u{cvtpk(o2.x, o2.x), cvtpk(o2.y, o2.y)};
  } else {
    __syncthreads();
    SB();
#pragma unroll
    for (int i = NPRE; i < 19; ++i) issue(i);
    STAMP(5);
  }
  f32x4 pa = mfma_rows(L.hv[rho], sel, ld[18], f32x4{0.f, 0.f, 0.f, 0.f});
  asm volatile("" : "+v"(pa));
  SB();
  __syncthreads();
  SB();
  {
    const v2u oc = *(const v2u*)(L.o + 2 * rho);
    pa = mfma_rows(oc.x, sel, ld[16], pa);
    const f32x4 pb = mfma_rows(oc.y, sel, ld[17], f32x4{0.f, 0.f, 0.f, 0.f});
    if (lane < 32) *(f32x4*)(L.p + w * 128 + 8 * c + 4 * r) = pa + pb;
  }
  __syncthreads();
  STAMP(6);
  if (tid < 40) {
    f32x2 a[NWAVE];
#pragma unroll
    for (int t = 0; t < NWAVE; ++t) a[t] = *(const f32x2*)(L.p + t * 128 + 2 * tid);
#pragma unroll
    for (int st = 1; st < NWAVE; st *= 2)
#pragma unroll
      for (int t = 0; t < NWAVE; t += 2 * st) a[t] += a[t + st];
    put2<PST>(rw, (tid >> 3) * 4096 + (tid & 7) * 16, WS_PG + s * 5 * 4096 + h * 128, a[0].x, a[0].y, tag32, tag8);
  }
#define NPOLL 1
#define PSTEP 4
  if (w >= 2 && w < 2 + NPOLL && h < 5) {
    if (NPOLL > 1) { if (w == 3) __builtin_amdgcn_s_sleep(PSTEP); else if (w == 4) __builtin_amdgcn_s_sleep(2 * PSTEP); else if (w == 5) __builtin_amdgcn_s_sleep(3 * PSTEP); }
    const int col = 80 * s + 16 * h + 2 * (lane & 7);
    const unsigned xv = *(gcu32*)((gcu16*)x_ + col);
    const unsigned long long enext = (((unsigned long long)L.tag[3] << 32) | L.tag[2]) + 1ull;
    auto finish = [&](f32x2 a) __attribute__((always_inline)) {
      a.x += dpp<ROR8>(a.x); a.y += dpp<ROR8>(a.y);
      a.x = fold16(a.x, a.y);
      a.x = fold32(a.x, a.x);
      const float xr = r == 0 ? bf_lo(xv) : bf_hi(xv);
      if (lane < 32 && c < 8) ((GAS unsigned short*)out_)[col + r] = (unsigned short)cvtpk(xr + a.x, 0.f);
      if (b == 0 && lane == 0) __hip_atomic_store(ws, enext, RLX_AGENT);
    };
    const f32x2 fsum = gather_sum<4, PLD>(rw, lane * 16, WS_PG + (s * 5 + h) * 4096, 1024, tag32, tag8);
    if (NPOLL > 1) {
      unsigned old = 0;
      if (lane == 0) old = __hip_atomic_fetch_add((LAS unsigned*)&L.xflag, 1u, RLX_WG);
      if (__builtin_amdgcn_readfirstlane(old) == 1u) finish(fsum);
    } else finish(fsum);
  }
  asm volatile(".Lkcode_end:" ::: "memory");
}

extern "C" void kernel_launch(void* const* d_in, const int* in_sizes, int n_in, void* d_out, int out_size, void* d_ws,
                              size_t ws_size, hipStream_t stream) {
  (void)in_sizes; (void)n_in; (void)out_size; (void)ws_size;
  hipLaunchKernelGGL(fused_decode, dim3(256), dim3(NT), 0, stream, (const unsigned short*)d_in[0], (const unsigned short*)d_in[1],
                     (const unsigned short*)d_in[2], (const unsigned short*)d_in[3], (const unsigned short*)d_in[4],
                     (const unsigned short*)d_in[6], (const unsigned short*)d_in[7], (const unsigned short*)d_in[8],
                     (unsigned short*)d_out, (unsigned long long*)((char*)d_ws + WSOFF));
}
```

```cpp
#define TOUCH 4096
#include <hip/hip_runtime.h>
#include <hip/hip_bf16.h>
#include <stdint.h>

#define GAS __attribute__((address_space(1)))
#define LAS __attribute__((address_space(3)))
typedef unsigned v4u __attribute__((ext_vector_type(4)));
typedef unsigned v2u __attribute__((ext_vector_type(2)));
typedef float f32x2 __attribute__((ext_vector_type(2)));
typedef float f32x4 __attribute__((ext_vector_type(4)));
typedef __attribute__((ext_vector_type(2))) __bf16 bf16x2;
typedef __attribute__((ext_vector_type(8))) __bf16 bf16x8;
typedef GAS unsigned gu32;
typedef GAS unsigned long long gu64;
typedef GAS const unsigned short gcu16;
typedef GAS const unsigned gcu32;
typedef GAS const v4u gcv4u;
#define RLX_AGENT __ATOMIC_RELAXED, __HIP_MEMORY_SCOPE_AGENT
#define RLX_WG __ATOMIC_RELAXED, __HIP_MEMORY_SCOPE_WORKGROUP

constexpr int D = 640, HQ = 32, DH = 128, SK = 32768, WIN = 4096, F = 1728;
constexpr int NSPLIT = 8, KPW = WIN / NSPLIT;
#define WSOFF 0
constexpr int NT = 1024, NWAVE = 16;
constexpr int NFG = F / 64;
constexpr unsigned SPIN_CAP = 1u << 16;

constexpr unsigned WS_QG = 1024;
constexpr unsigned WS_FG = WS_QG + HQ * NSPLIT * 1024;
constexpr unsigned WS_OG = WS_FG + HQ * NSPLIT * 1024;
constexpr unsigned WS_SG = WS_OG + HQ * NSPLIT * 1024;
constexpr unsigned WS_PG = WS_SG + HQ * 128;
constexpr unsigned WS_END = WS_PG + 8 * 5 * 32 * 128;

__device__ __forceinline__ float bf_lo(unsigned w) { return __builtin_bit_cast(float, w << 16); }
__device__ __forceinline__ float bf_hi(unsigned w) { return __builtin_bit_cast(float, w & 0xffff0000u); }
__device__ __forceinline__ unsigned cvtpk(float lo, float hi) {
  unsigned r;
  asm("v_cvt_pk_bf16_f32 %0, %1, %2" : "=v"(r) : "v"(lo), "v"(hi));
  return r;
}
__device__ __forceinline__ float rbf(float x) { return bf_lo(cvtpk(x, x)); }
__device__ __forceinline__ float dot2(unsigned a, unsigned b, float acc) {
  return __builtin_amdgcn_fdot2_f32_bf16(__builtin_bit_cast(bf16x2, a), __builtin_bit_cast(bf16x2, b), acc, false);
}
__device__ __forceinline__ f32x4 mfma_rows(unsigned pk, const v4u& sel, const v4u& wreg, f32x4 acc) {
  const v4u a = {pk & sel.x, pk & sel.y, pk & sel.z, pk & sel.w};
  return __builtin_amdgcn_mfma_f32_16x16x32_bf16(__builtin_bit_cast(bf16x8, a), __builtin_bit_cast(bf16x8, wreg), acc, 0, 0, 0);
}
template <int CTRL> __device__ __forceinline__ float dpp(float x) {
  return __builtin_bit_cast(float, __builtin_amdgcn_mov_dpp(__builtin_bit_cast(int, x), CTRL, 0xf, 0xf, true));
}
constexpr int XOR1 = 0xB1, XOR2 = 0x4E, HMIR = 0x141, ROR8 = 0x128;
__device__ __forceinline__ float sum8(float x) { x += dpp<XOR1>(x); x += dpp<XOR2>(x); x += dpp<HMIR>(x); return x; }
__device__ __forceinline__ float max8(float x) { x = fmaxf(x, dpp<XOR1>(x)); x = fmaxf(x, dpp<XOR2>(x)); x = fmaxf(x, dpp<HMIR>(x)); return x; }
__device__ __forceinline__ float row_sum(float x) { x = sum8(x); x += dpp<ROR8>(x); return x; }
__device__ __forceinline__ float fold32(float a, float b) {
  auto t = __builtin_amdgcn_permlane32_swap(__float_as_uint(a), __float_as_uint(b), false, false);
  return __uint_as_float(t[0]) + __uint_as_float(t[1]);
}
__device__ __forceinline__ float fold16(float a, float b) {
  auto t = __builtin_amdgcn_permlane16_swap(__float_as_uint(a), __float_as_uint(b), false, false);
  return __uint_as_float(t[0]) + __uint_as_float(t[1]);
}
__device__ __forceinline__ float xrow_sum(float x) { x = fold16(x, x); return fold32(x, x); }
__device__ __forceinline__ float xrow_max(float x) {
  auto s = __builtin_amdgcn_permlane16_swap(__float_as_uint(x), __float_as_uint(x), false, false);
  x = fmaxf(__uint_as_float(s[0]), __uint_as_float(s[1]));
  auto t = __builtin_amdgcn_permlane32_swap(__float_as_uint(x), __float_as_uint(x), false, false);
  return fmaxf(__uint_as_float(t[0]), __uint_as_float(t[1]));
}
__device__ __forceinline__ float wave_sum(float x) { return xrow_sum(row_sum(x)); }
__device__ __forceinline__ f32x2 fold8(const float (&v)[8]) {
  float t0 = fold32(v[0], v[4]), t1 = fold32(v[1], v[5]), t2 = fold32(v[2], v[6]), t3 = fold32(v[3], v[7]);
  f32x2 u;
  u.x = fold16(t0, t2);
  u.y = fold16(t1, t3);
  return u;
}

__device__ __forceinline__ v4u ld_nt(gcv4u* p) { return __builtin_nontemporal_load(p); }
typedef __amdgpu_buffer_rsrc_t rsrc_t;
__device__ __forceinline__ rsrc_t mk(const void* p, unsigned bytes) { return __builtin_amdgcn_make_buffer_rsrc((void*)p, 0, bytes, 0x00020000); }
template <int AUX> __device__ __forceinline__ v4u bld(rsrc_t r, unsigned voff, unsigned soff) { return __builtin_amdgcn_raw_buffer_load_b128(r, voff, soff, AUX); }
#define SB() __builtin_amdgcn_sched_barrier(0)
#define QST 16
#define QLD 16
#define FST 16
#define FLD 16
#define SST 16
#define OST 16
#define OLD 16
#define PST 16
#define PLD 16
#define PRE 0
#define SCAUX 16
constexpr int SC1 = SCAUX;
template <int AUX>
__device__ __forceinline__ void put2(rsrc_t rw, unsigned voff, unsigned soff, float a, float b, unsigned tag32, unsigned tag8) {
  const v4u g = {((__float_as_uint(a) + 8u) & 0xfffffff0u) | tag8, tag32, ((__float_as_uint(b) + 8u) & 0xfffffff0u) | tag8, tag32};
  __builtin_amdgcn_raw_buffer_store_b128(g, rw, voff, soff, AUX);
}
struct TagChk {
  unsigned b32 = 0, b8 = 0;
  __device__ __forceinline__ f32x2 take(const v4u& x, unsigned tag32, unsigned tag8) {
    b32 |= __builtin_amdgcn_bitop3_b32(x.y, x.w, tag32, 0x7e);
    const unsigned a = x.x ^ tag8, b = x.z ^ tag8;
    b8 |= a | b;
    return f32x2{__uint_as_float(a), __uint_as_float(b)};
  }
  __device__ __forceinline__ bool ok() const { return __all((b32 | (b8 & 0xfu)) == 0u); }
};
template <int N, int AUX>
__device__ __forceinline__ f32x2 gather_sum(rsrc_t rw, unsigned voff, unsigned soff, unsigned stride, unsigned tag32, unsigned tag8) {
  f32x2 sum;
#pragma nounroll
  for (unsigned spins = 0; spins < SPIN_CAP; ++spins) {
    v4u x[N];
#pragma unroll
    for (int k = 0; k < N; ++k) x[k] = bld<AUX>(rw, voff, soff + k * stride);
    SB();
    TagChk tc;
    f32x2 v[N];
#pragma unroll
    for (int k = 0; k < N; ++k) v[k] = tc.take(x[k], tag32, tag8);
    sum = v[0];
#pragma unroll
    for (int k = 1; k < N; ++k) sum += v[k];
    asm volatile("" : "+v"(sum));
    if (tc.ok()) return sum;
    __builtin_amdgcn_s_sleep(1);
  }
  return f32x2{__uint_as_float(0x7fc00000u), __uint_as_float(0x7fc00000u)};
}
#define STAMP(k)
constexpr int NTA = 2;
#define KAUX 2
#define VAUX 2
#define WAUX 2

template <int N> struct IC { static constexpr int value = N; };
struct Lds {
  unsigned q[64];
  unsigned xn[40];
  unsigned tag[4];
  unsigned xflag, qflag, mcnt, gcnt;
  float ml[NWAVE * 2];
  float acc[NWAVE * 128];
  float p[NWAVE * 128];
  float gu[10 * 128];
  unsigned o[128];
  unsigned hv[64];
};
template <int N> __device__ __forceinline__ void waitvm() { asm volatile("s_waitcnt vmcnt(%0)" ::"i"(N) : "memory"); }
template <int A, int B> __device__ __forceinline__ void waitvm2(int w) {
  asm volatile("s_cmp_eq_u32 %0, 0\n\ts_cbranch_scc1 1f\n\ts_waitcnt vmcnt(%2)\n1:\n\ts_waitcnt vmcnt(%1)" ::"s"(w), "i"(A), "i"(B) : "scc", "memory");
}
__device__ __forceinline__ void lds_wait(unsigned* word, unsigned want) {
  unsigned spins = 0;
  while (__builtin_amdgcn_readfirstlane(__hip_atomic_load((LAS unsigned*)word, RLX_WG)) != want && ++spins < (1u << 22)) __builtin_amdgcn_s_sleep(2);
  asm volatile("" ::: "memory");
}
#define WND 2
#define WND0 4
#define JX 1
#define JS 14
#define JG 4
#define JQ 10
#define NPRE 19
#define JW 99
#define WND2 3
#define JH 12
#define HW 0
#define JF 6
#define PRIO 0
constexpr int nissued(int j) { const int n = j + 1 + (j < JW ? WND : WND2); return n < NPRE ? n : NPRE; }
#define ROTM 7

__global__ void __launch_bounds__(NT, 4)
fused_decode(const unsigned short* __restrict__ x_, const unsigned short* __restrict__ kc_, const unsigned short* __restrict__ vc_,
             const unsigned short* __restrict__ wq_, const unsigned short* __restrict__ wo_, const unsigned short* __restrict__ wg_,
             const unsigned short* __restrict__ wu_, const unsigned short* __restrict__ wd_, unsigned short* __restrict__ out_,
             unsigned long long* ws_) {
  __shared__ __attribute__((aligned(16))) Lds L;
  constexpr float LOG2E = 1.4426950408889634f;
  const int b = blockIdx.x, xcd = b & 7, bi = b >> 3;
  const int h = (bi >> 3) * 8 + xcd, s = bi & 7;
  const int tid = threadIdx.x, lane = tid & 63, w = __builtin_amdgcn_readfirstlane(tid >> 6), r = lane >> 4, c = lane & 15;
  const int rr = lane >> 3;
  const bool ffn = h < NFG;
  gu64* ws = (gu64*)ws_;
  const rsrc_t rw = mk(ws_, WS_END);
  v4u cpf;
  {
    unsigned long long pc;
    unsigned clen;
    asm volatile("s_getpc_b64 %0\n.Lkcode_begin:\n\ts_mov_b32 %1, .Lkcode_end-.Lkcode_begin" : "=s"(pc), "=s"(clen));
    const rsrc_t rc = mk((const void*)pc, clen);
    cpf = bld<0>(rc, tid * 16, 0);
    SB();
  }
  asm volatile("" ::"s"(x_), "s"(kc_), "s"(vc_), "s"(wq_), "s"(wo_), "s"(wg_), "s"(wu_), "s"(wd_), "s"(out_), "s"(ws_));
  STAMP(0);
  const rsrc_t rk = mk(kc_ + ((size_t)h * SK + (SK - WIN) + s * KPW + 32 * w) * DH, 32 * DH * 2);
  const rsrc_t rv = mk(vc_ + ((size_t)h * SK + (SK - WIN) + s * KPW + 32 * w) * DH, 32 * DH * 2);
  const rsrc_t ro = mk(wo_ + (size_t)(h * DH) * D + 80 * s, DH * D * 2 - 160 * s);
  const rsrc_t rd = mk(wd_ + (size_t)((ffn ? h : 0) * 64) * D + 80 * s, ffn ? 64 * D * 2 - 160 * s : 0);
  const unsigned ooff = c < 10 ? r * (D * 2) + 16 * c : 0x40000000u;
  const unsigned ooff2 = c < 10 ? r * (2 * D * 2) + 16 * c : 0x40000000u;
  v4u sel;
  {
    const unsigned hm = c < 8 ? ((c & 1) ? 0xffff0000u : 0x0000ffffu) : 0u;
    sel = v4u{(c >> 1) == 0 ? hm : 0u, (c >> 1) == 1 ? hm : 0u, (c >> 1) == 2 ? hm : 0u, (c >> 1) == 3 ? hm : 0u};
  }
  const int rho = 4 * w + r;
  const int rot = (int)(((unsigned)(b * 16 + w) * 2654435761u) >> 16) & ROTM;
  v4u ld[19];
  auto issue = [&](int i) __attribute__((always_inline)) {
    if (i < 8) ld[i] = bld<KAUX>(rk, lane * 16, ((i + rot) & 7) * 1024);
    else if (i < 16) ld[i] = bld<VAUX>(rv, lane * 16, ((i + rot) & 7) * 1024);
    else if (i == 16) ld[i] = bld<WAUX>(ro, ooff2, w * (8 * D * 2));
    else if (i == 17) ld[i] = bld<WAUX>(ro, ooff2, w * (8 * D * 2) + D * 2);
    else if (i == 18) ld[i] = bld<WAUX>(rd, ooff, w * (4 * D * 2));
    SB();
  };
  v4u gv, uv;
  if (w == 0) {
    __builtin_amdgcn_s_setprio(3);
    const rsrc_t rx = mk(x_, D * 2);
    const v4u a4 = bld<0>(rx, 20 * lane, 0);
    const unsigned a1 = __builtin_amdgcn_raw_buffer_load_b32(rx, 20 * lane + 16, 0, 0);
    const v4u b4 = bld<0>(rx, 20 * rr, 160 * s);
    const unsigned b1 = __builtin_amdgcn_raw_buffer_load_b32(rx, 20 * rr + 16, 160 * s, 0);
    const unsigned long long ep = __hip_atomic_load(ws, RLX_AGENT);
    SB();
    constexpr int QS = HQ * DH * 2;
    const rsrc_t rq = mk(wq_ + (size_t)(80 * s) * (HQ * DH) + h * DH, 79 * QS + 256);
    const unsigned woff = rr * 10 * QS + 16 * (lane & 7);
    v4u wv[20];
#pragma unroll
    for (int i = 0; i < 20; ++i) { wv[i] = bld<WAUX>(rq, woff + (i & 1) * 128, (i >> 1) * QS); SB(); }
    if (lane == 0) { L.xflag = 0u; L.qflag = 0u; L.mcnt = 0u; L.gcnt = 0u; }
    __syncthreads();
    SB();
    const unsigned e_lo = __builtin_amdgcn_readfirstlane((unsigned)ep), e_hi = __builtin_amdgcn_readfirstlane((unsigned)(ep >> 32));
    unsigned tag32 = (e_lo + 1u) * 0x9E3779B1u; tag32 ^= tag32 >> 15; tag32 *= 0x85EBCA77u; tag32 ^= tag32 >> 13; tag32 ^= 0x5bd1e995u;
    const unsigned tag8 = (((e_hi ^ (e_lo * 0xC2B2AE35u)) * 0x27D4EB2Fu) >> 28) ^ 0xAu;
    float xs[10];
#pragma unroll
    for (int i = 0; i < 4; ++i) { xs[2 * i] = bf_lo(a4[i]); xs[2 * i + 1] = bf_hi(a4[i]); }
    xs[8] = bf_lo(a1); xs[9] = bf_hi(a1);
    float sum = 0.f;
#pragma unroll
    for (int i = 0; i < 10; ++i) sum += xs[i];
    const float mean = wave_sum(sum) * (1.f / D);
    float sq = 0.f;
#pragma unroll
    for (int i = 0; i < 10; ++i) { const float d = xs[i] - mean; sq = fmaf(d, d, sq); }
    const float var = wave_sum(sq) * (1.f / D);
    const float mean_b = rbf(mean);
    const float den = rbf(sqrtf(rbf(rbf(var) + 1e-5f)));
    const unsigned xb[5] = {b4[0], b4[1], b4[2], b4[3], b1};
    unsigned xp[5];
#pragma unroll
    for (int i = 0; i < 5; ++i) xp[i] = cvtpk(rbf(bf_lo(xb[i]) - mean_b) / den, rbf(bf_hi(xb[i]) - mean_b) / den);
    if ((lane & 7) == 0) {
#pragma unroll
      for (int i = 0; i < 5; ++i) L.xn[5 * rr + i] = xp[i];
    }
    if (lane == 0) { L.tag[0] = tag32; L.tag[1] = tag8; L.tag[2] = e_lo; L.tag[3] = e_hi; }
    asm volatile("s_waitcnt lgkmcnt(0)" ::: "memory");
    if (lane == 0) __hip_atomic_store((LAS unsigned*)&L.xflag, 1u, RLX_WG);
    float accA[8], accB[8];
#pragma unroll
    for (int k = 0; k < 8; ++k) { accA[k] = 0.f; accB[k] = 0.f; }
#pragma unroll
    for (int i = 0; i < 10; ++i) {
      const unsigned xlo = (i & 1) ? xp[i >> 1] >> 16 : xp[i >> 1] & 0xffffu, xhi = (i & 1) ? xp[i >> 1] & 0xffff0000u : xp[i >> 1] << 16;
#pragma unroll
      for (int k = 0; k < 4; ++k) {
        accA[2 * k] = dot2(wv[2 * i][k], xlo, accA[2 * k]);
        accA[2 * k + 1] = dot2(wv[2 * i][k], xhi, accA[2 * k + 1]);
        accB[2 * k] = dot2(wv[2 * i + 1][k], xlo, accB[2 * k]);
        accB[2 * k + 1] = dot2(wv[2 * i + 1][k], xhi, accB[2 * k + 1]);
      }
    }
    f32x2 ua = fold8(accA), ub = fold8(accB);
    ua.x += dpp<ROR8>(ua.x); ua.y += dpp<ROR8>(ua.y); ub.x += dpp<ROR8>(ub.x); ub.y += dpp<ROR8>(ub.y);
    const f32x2 u = c < 8 ? ua : ub;
    put2<QST>(rw, lane * 16, WS_QG + (h * NSPLIT + s) * 1024, u.x, u.y, tag32, tag8);
    STAMP(1);
  } else {
    __syncthreads();
    if (PRIO == 1) { if (w >= 12) __builtin_amdgcn_s_setprio(3); else if (w >= 8) __builtin_amdgcn_s_setprio(2); else if (w >= 4) __builtin_amdgcn_s_setprio(1); }
    if (PRIO == 2) { if (w >= 8) __builtin_amdgcn_s_setprio(1); }
  }
  {
    const unsigned gbytes = __builtin_amdgcn_readfirstlane((ffn && w >= 1 && w <= 10) ? 7 * F * 2 + 128 : 0);
    const size_t goff = (size_t)(80 * s + 8 * ((w + 15) & 15)) * F + 64 * (ffn ? h : 0);
    const bool gon = w >= 1 && w <= 10;
    const rsrc_t rg = mk(wg_ + (gon ? goff : 0), gbytes), ru = mk(wu_ + (gon ? goff : 0), gbytes);
    SB();
    gv = bld<WAUX>(rg, rr * (F * 2) + 16 * (lane & 7), 0);
    uv = bld<WAUX>(ru, rr * (F * 2) + 16 * (lane & 7), 0);
    SB();
  }
  unsigned tch;
  {
    const char* tb; unsigned tn;
    if (w == 11) { tb = (const char*)(vc_ + ((size_t)h * SK + (SK - WIN) + s * KPW) * DH); tn = KPW * DH * 2; }
    else if (w == 12) { tb = (const char*)(wo_ + (size_t)(h * DH) * D); tn = DH * D * 2; }
    else if (w == 13) { tb = (const char*)(wd_ + (size_t)((ffn ? h : 0) * 64) * D); tn = 64 * D * 2; }
    else if (w == 15) { tb = (const char*)out_; tn = D * 2; }
    else { tb = (const char*)x_; tn = 0; }
    const rsrc_t rt = mk(tb, __builtin_amdgcn_readfirstlane(tn));
    unsigned tv = lane * TOUCH;
    tch = __builtin_amdgcn_raw_buffer_load_b32(rt, tv, 0, NTA);
    SB();
  }
  v4u qp;
  unsigned tag32 = 0, tag8 = 0;
  float sc[8], m_w = 0.f;
  unsigned pk[8];
  f32x4 acc = {0.f, 0.f, 0.f, 0.f};
  auto qk = [&](int i) __attribute__((always_inline)) {
    constexpr float SCALE = 0.08838834764831845f;
    float d = 0.f;
#pragma unroll
    for (int k = 0; k < 4; ++k) d = dot2(ld[i][k], qp[k], d);
    sc[i] = rbf(rbf(row_sum(d)) * SCALE);
  };
  auto pv = [&](int i) __attribute__((always_inline)) {
    acc = mfma_rows(pk[i], sel, ld[8 + i], acc);
    asm volatile("" : "+v"(acc));
  };
#pragma unroll
  for (int i = 0; i < WND; ++i) issue(i);
  auto step = [&](auto J) __attribute__((always_inline)) {
    constexpr int j = decltype(J)::value;
    if constexpr (nissued(j) > nissued(j - 1)) {
      waitvm2<nissued(j - 1) - j - 1 + WND0 - WND, nissued(j - 1) - j - 1>(w);
#pragma unroll
      for (int i = nissued(j - 1); i < nissued(j); ++i) issue(i);
    }
    if constexpr (j == JX) if (w == 0) {
      const unsigned t32 = __builtin_amdgcn_readfirstlane(L.tag[0]), t8 = __builtin_amdgcn_readfirstlane(L.tag[1]);
      const f32x2 qs = gather_sum<8, QLD>(rw, lane * 16, WS_QG + h * NSPLIT * 1024, 1024, t32, t8);
      const float q0 = qs.x, q1 = qs.y;
      L.q[4 * c + r] = cvtpk(q0, q1);
      asm volatile("s_waitcnt lgkmcnt(0)" ::: "memory");
      if (lane == 0) __hip_atomic_store((LAS unsigned*)&L.qflag, 1u, RLX_WG);
      STAMP(8);
      SB();
    }
    if constexpr (j == JG) {
      asm volatile("" ::"v"(cpf));
      asm volatile("" ::"v"(tch));
      lds_wait(&L.xflag, 1u);
      tag32 = __builtin_amdgcn_readfirstlane(L.tag[0]); tag8 = __builtin_amdgcn_readfirstlane(L.tag[1]);
      if (ffn && w >= 1 && w <= 10) {
        const unsigned xw = L.xn[4 * (w - 1) + (rr >> 1)];
        const float xn = (rr & 1) ? bf_hi(xw) : bf_lo(xw);
        float g[8], u[8];
#pragma unroll
        for (int k = 0; k < 4; ++k) {
          g[2 * k] = xn * bf_lo(gv[k]); g[2 * k + 1] = xn * bf_hi(gv[k]);
          u[2 * k] = xn * bf_lo(uv[k]); u[2 * k + 1] = xn * bf_hi(uv[k]);
        }
        f32x2 gs = fold8(g), us = fold8(u);
        gs.x += dpp<ROR8>(gs.x); gs.y += dpp<ROR8>(gs.y); us.x += dpp<ROR8>(us.x); us.y += dpp<ROR8>(us.y);
        *(f32x2*)(L.gu + (w - 1) * 128 + 8 * c + 2 * r) = c < 8 ? gs : us;
        asm volatile("s_waitcnt lgkmcnt(0)" ::: "memory");
        if (lane == 0) __hip_atomic_fetch_add((LAS unsigned*)&L.gcnt, 1u, RLX_WG);
      }
      SB();
    }
    if constexpr (j == JF) if (w == 0 && ffn) {
      lds_wait(&L.gcnt, 10u);
      float gg = 0.f, uu = 0.f;
#pragma unroll
      for (int t = 0; t < 10; ++t) { gg += L.gu[t * 128 + lane]; uu += L.gu[t * 128 + 64 + lane]; }
      put2<FST>(rw, lane * 16, WS_FG + (h * NSPLIT + s) * 1024, gg, uu, tag32, tag8);
      SB();
    }
    if constexpr (j == JQ) {
      STAMP(2);
      lds_wait(&L.qflag, 1u);
      qp = *(const v4u*)(L.q + 4 * c);
#pragma unroll
      for (int i = 0; i < 4; ++i) qk(i);
      SB();
    }
    if constexpr (j == JQ + 1) {
#pragma unroll
      for (int i = 4; i < 8; ++i) qk(i);
      float m = sc[0];
#pragma unroll
      for (int i = 1; i < 8; ++i) m = fmaxf(m, sc[i]);
      m_w = xrow_max(m);
      float l = 0.f;
#pragma unroll
      for (int i = 0; i < 8; ++i) { const float pe = __builtin_amdgcn_exp2f((sc[i] - m_w) * LOG2E); l += pe; pk[i] = cvtpk(pe, pe); }
      const float l_w = xrow_sum(l);
      if (lane == 0) {
        *(f32x2*)(L.ml + 2 * w) = f32x2{m_w, l_w};
        asm volatile("s_waitcnt lgkmcnt(0)" ::: "memory");
        __hip_atomic_fetch_add((LAS unsigned*)&L.mcnt, 1u, RLX_WG);
      }
      STAMP(3);
      SB();
    }
    if constexpr (j == JQ + 2) {
#pragma unroll
      for (int i = 0; i <= j - 8; ++i) pv(i);
      SB();
    }
    if constexpr (j > JQ + 2) { pv(j - 8); SB(); }
    if constexpr (j == JH) if (w == HW) {
      float hv = 0.f;
      if (ffn) {
        const f32x2 gu = gather_sum<8, FLD>(rw, lane * 16, WS_FG + h * NSPLIT * 1024, 1024, tag32, tag8);
        float g = gu.x, u = gu.y;
        g = rbf(g); u = rbf(u);
        const float sg = rbf(1.f / (1.f + __builtin_amdgcn_exp2f(-g * LOG2E)));
        hv = rbf(sg * u);
      }
      L.hv[lane] = cvtpk(hv, hv);
      STAMP(9);
    }
  };
  step(IC<0>{});  step(IC<1>{});  step(IC<2>{});  step(IC<3>{});
  step(IC<4>{});  step(IC<5>{});  step(IC<6>{});  step(IC<7>{});
  step(IC<8>{});  step(IC<9>{});  step(IC<10>{});  step(IC<11>{});
  step(IC<12>{});  step(IC<13>{});  step(IC<14>{});  step(IC<15>{});
#pragma unroll
  for (int i = nissued(15); i < NPRE; ++i) issue(i);
  if (lane < 32) *(f32x4*)(L.acc + w * 128 + 8 * c + 4 * r) = acc;
  STAMP(4);
  if (w == 0) {
    lds_wait(&L.mcnt, (unsigned)NWAVE);
    const f32x2 ml = *(const f32x2*)(L.ml + 2 * c);
    const float m8 = max8(ml.x), M = fmaxf(m8, dpp<ROR8>(m8));
    const float fl = __builtin_amdgcn_exp2f((ml.x - M) * LOG2E);
    const float Ls = row_sum(fl * ml.y);
    put2<SST>(rw, lane == 0 ? 0u : 0x08000000u, WS_SG + h * 128 + s * 16, M, Ls, tag32, tag8);
    float fw[NWAVE];
#pragma unroll
    for (int t = 0; t < NWAVE; ++t) fw[t] = __builtin_bit_cast(float, __builtin_amdgcn_readlane(__builtin_bit_cast(int, fl), t));
    STAMP(11);
    SB();
    __syncthreads();
    SB();
    f32x2 a[NWAVE];
#pragma unroll
    for (int t = 0; t < NWAVE; ++t) a[t] = *(const f32x2*)(L.acc + t * 128 + 2 * lane);
    f32x2 oa = a[0] * fw[0], ob = a[1] * fw[1];
#pragma unroll
    for (int t = 2; t < NWAVE; t += 2) { oa += a[t] * fw[t]; ob += a[t + 1] * fw[t + 1]; }
    const float o0 = oa.x + ob.x, o1 = oa.y + ob.y;
    put2<OST>(rw, lane * 16, WS_OG + (h * NSPLIT + s) * 1024, o0, o1, tag32, tag8);
    SB();
#pragma unroll
    for (int i = NPRE; i < 19; ++i) issue(i);
    STAMP(5);
    f32x2 o2 = {__uint_as_float(0x7fc00000u), __uint_as_float(0x7fc00000u)};
#pragma nounroll
    for (unsigned spins = 0; spins < SPIN_CAP; ++spins) {
      const v4u xs = bld<OLD>(rw, (lane & 7) * 16, WS_SG + h * 128);
      v4u x[8];
#pragma unroll
      for (int k = 0; k < 8; ++k) x[k] = bld<OLD>(rw, lane * 16, WS_OG + (h * NSPLIT + k) * 1024);
      SB();
      TagChk tc;
      const f32x2 st = tc.take(xs, tag32, tag8);
      const float MM = max8(st.x);
      const float fs = __builtin_amdgcn_exp2f((st.x - MM) * LOG2E);
      const float ws8 = fs * __builtin_amdgcn_rcpf(sum8(fs * st.y));
      float wk[8];
#pragma unroll
      for (int k = 0; k < 8; ++k) wk[k] = __builtin_bit_cast(float, __builtin_amdgcn_readlane(__builtin_bit_cast(int, ws8), k));
      SB();
      f32x2 acc2 = tc.take(x[0], tag32, tag8) * wk[0];
#pragma unroll
      for (int k = 1; k < 8; ++k) acc2 += tc.take(x[k], tag32, tag8) * wk[k];
      asm volatile("" : "+v"(acc2));
      if (tc.ok()) { o2 = acc2; break; }
      __builtin_amdgcn_s_sleep(1);
    }
    STAMP(10);
    *(v2u*)(L.o + 2 * lane) = v2u{cvtpk(o2.x, o2.x), cvtpk(o2.y, o2.y)};
  } else {
    __syncthreads();
    SB();
#pragma unroll
    for (int i = NPRE; i < 19; ++i) issue(i);
    STAMP(5);
  }
  f32x4 pa = mfma_rows(L.hv[rho], sel, ld[18], f32x4{0.f, 0.f, 0.f, 0.f});
  asm volatile("" : "+v"(pa));
  SB();
  __syncthreads();
  SB();
  {
    const v2u oc = *(const v2u*)(L.o + 2 * rho);
    pa = mfma_rows(oc.x, sel, ld[16], pa);
    const f32x4 pb = mfma_rows(oc.y, sel, ld[17], f32x4{0.f, 0.f, 0.f, 0.f});
    if (lane < 32) *(f32x4*)(L.p + w * 128 + 8 * c + 4 * r) = pa + pb;
  }
  __syncthreads();
  STAMP(6);
  if (tid < 40) {
    f32x2 a[NWAVE];
#pragma unroll
    for (int t = 0; t < NWAVE; ++t) a[t] = *(const f32x2*)(L.p + t * 128 + 2 * tid);
#pragma unroll
    for (int st = 1; st < NWAVE; st *= 2)
#pragma unroll
      for (int t = 0; t < NWAVE; t += 2 * st) a[t] += a[t + st];
    put2<PST>(rw, (tid >> 3) * 4096 + (tid & 7) * 16, WS_PG + s * 5 * 4096 + h * 128, a[0].x, a[0].y, tag32, tag8);
  }
#define NPOLL 1
#define PSTEP 4
  if (w >= 2 && w < 2 + NPOLL && h < 5) {
    if (NPOLL > 1) { if (w == 3) __builtin_amdgcn_s_sleep(PSTEP); else if (w == 4) __builtin_amdgcn_s_sleep(2 * PSTEP); else if (w == 5) __builtin_amdgcn_s_sleep(3 * PSTEP); }
    const int col = 80 * s + 16 * h + 2 * (lane & 7);
    const unsigned xv = *(gcu32*)((gcu16*)x_ + col);
    const unsigned long long enext = (((unsigned long long)L.tag[3] << 32) | L.tag[2]) + 1ull;
    auto finish = [&](f32x2 a) __attribute__((always_inline)) {
      a.x += dpp<ROR8>(a.x); a.y += dpp<ROR8>(a.y);
      a.x = fold16(a.x, a.y);
      a.x = fold32(a.x, a.x);
      const float xr = r == 0 ? bf_lo(xv) : bf_hi(xv);
      if (lane < 32 && c < 8) ((GAS unsigned short*)out_)[col + r] = (unsigned short)cvtpk(xr + a.x, 0.f);
      if (b == 0 && lane == 0) __hip_atomic_store(ws, enext, RLX_AGENT);
    };
    const f32x2 fsum = gather_sum<4, PLD>(rw, lane * 16, WS_PG + (s * 5 + h) * 4096, 1024, tag32, tag8);
    if (NPOLL > 1) {
      unsigned old = 0;
      if (lane == 0) old = __hip_atomic_fetch_add((LAS unsigned*)&L.xflag, 1u, RLX_WG);
      if (__builtin_amdgcn_readfirstlane(old) == 1u) finish(fsum);
    } else finish(fsum);
  }
  asm volatile(".Lkcode_end:" ::: "memory");
}

extern "C" void kernel_launch(void* const* d_in, const int* in_sizes, int n_in, void* d_out, int out_size, void* d_ws,
                              size_t ws_size, hipStream_t stream) {
  (void)in_sizes; (void)n_in; (void)out_size; (void)ws_size;
  hipLaunchKernelGGL(fused_decode, dim3(256), dim3(NT), 0, stream, (const unsigned short*)d_in[0], (const unsigned short*)d_in[1],
                     (const unsigned short*)d_in[2], (const unsigned short*)d_in[3], (const unsigned short*)d_in[4],
                     (const unsigned short*)d_in[6], (const unsigned short*)d_in[7], (const unsigned short*)d_in[8],
                     (unsigned short*)d_out, (unsigned long long*)((char*)d_ws + WSOFF));
}
```

```cpp
#include <hip/hip_runtime.h>
#include <hip/hip_bf16.h>
#include <stdint.h>

#define GAS __attribute__((address_space(1)))
#define LAS __attribute__((address_space(3)))
typedef unsigned v4u __attribute__((ext_vector_type(4)));
typedef unsigned v2u __attribute__((ext_vector_type(2)));
typedef float f32x2 __attribute__((ext_vector_type(2)));
typedef float f32x4 __attribute__((ext_vector_type(4)));
typedef __attribute__((ext_vector_type(2))) __bf16 bf16x2;
typedef __attribute__((ext_vector_type(8))) __bf16 bf16x8;
typedef GAS unsigned gu32;
typedef GAS unsigned long long gu64;
typedef GAS const unsigned short gcu16;
typedef GAS const unsigned gcu32;
typedef GAS const v4u gcv4u;
#define RLX_AGENT __ATOMIC_RELAXED, __HIP_MEMORY_SCOPE_AGENT
#define RLX_WG __ATOMIC_RELAXED, __HIP_MEMORY_SCOPE_WORKGROUP

constexpr int D = 640, HQ = 32, DH = 128, SK = 32768, WIN = 4096, F = 1728;
constexpr int NSPLIT = 8, KPW = WIN / NSPLIT;
#define WSOFF 0
constexpr int NT = 1024, NWAVE = 16;
constexpr int NFG = F / 64;
constexpr unsigned SPIN_CAP = 1u << 16;

constexpr unsigned WS_QG = 1024;
constexpr unsigned WS_FG = WS_QG + HQ * NSPLIT * 1024;
constexpr unsigned WS_OG = WS_FG + HQ * NSPLIT * 1024;
constexpr unsigned WS_SG = WS_OG + HQ * NSPLIT * 1024;
constexpr unsigned WS_PG = WS_SG + HQ * 128;
constexpr unsigned WS_END = WS_PG + 8 * 5 * 32 * 128;

__device__ __forceinline__ float bf_lo(unsigned w) { return __builtin_bit_cast(float, w << 16); }
__device__ __forceinline__ float bf_hi(unsigned w) { return __builtin_bit_cast(float, w & 0xffff0000u); }
__device__ __forceinline__ unsigned cvtpk(float lo, float hi) {
  unsigned r;
  asm("v_cvt_pk_bf16_f32 %0, %1, %2" : "=v"(r) : "v"(lo), "v"(hi));
  return r;
}
__device__ __forceinline__ float rbf(float x) { return bf_lo(cvtpk(x, x)); }
__device__ __forceinline__ float dot2(unsigned a, unsigned b, float acc) {
  return __builtin_amdgcn_fdot2_f32_bf16(__builtin_bit_cast(bf16x2, a), __builtin_bit_cast(bf16x2, b), acc, false);
}
__device__ __forceinline__ f32x4 mfma_rows(unsigned pk, const v4u& sel, const v4u& wreg, f32x4 acc) {
  const v4u a = {pk & sel.x, pk & sel.y, pk & sel.z, pk & sel.w};
  return __builtin_amdgcn_mfma_f32_16x16x32_bf16(__builtin_bit_cast(bf16x8, a), __builtin_bit_cast(bf16x8, wreg), acc, 0, 0, 0);
}
template <int CTRL> __device__ __forceinline__ float dpp(float x) {
  return __builtin_bit_cast(float, __builtin_amdgcn_mov_dpp(__builtin_bit_cast(int, x), CTRL, 0xf, 0xf, true));
}
constexpr int XOR1 = 0xB1, XOR2 = 0x4E, HMIR = 0x141, ROR8 = 0x128;
__device__ __forceinline__ float sum8(float x) { x += dpp<XOR1>(x); x += dpp<XOR2>(x); x += dpp<HMIR>(x); return x; }
__device__ __forceinline__ float max8(float x) { x = fmaxf(x, dpp<XOR1>(x)); x = fmaxf(x, dpp<XOR2>(x)); x = fmaxf(x, dpp<HMIR>(x)); return x; }
__device__ __forceinline__ float row_sum(float x) { x = sum8(x); x += dpp<ROR8>(x); return x; }
__device__ __forceinline__ float fold32(float a, float b) {
  auto t = __builtin_amdgcn_permlane32_swap(__float_as_uint(a), __float_as_uint(b), false, false);
  return __uint_as_float(t[0]) + __uint_as_float(t[1]);
}
__device__ __forceinline__ float fold16(float a, float b) {
  auto t = __builtin_amdgcn_permlane16_swap(__float_as_uint(a), __float_as_uint(b), false, false);
  return __uint_as_float(t[0]) + __uint_as_float(t[1]);
}
__device__ __forceinline__ float xrow_sum(float x) { x = fold16(x, x); return fold32(x, x); }
__device__ __forceinline__ float xrow_max(float x) {
  auto s = __builtin_amdgcn_permlane16_swap(__float_as_uint(x), __float_as_uint(x), false, false);
  x = fmaxf(__uint_as_float(s[0]), __uint_as_float(s[1]));
  auto t = __builtin_amdgcn_permlane32_swap(__float_as_uint(x), __float_as_uint(x), false, false);
  return fmaxf(__uint_as_float(t[0]), __uint_as_float(t[1]));
}
__device__ __forceinline__ float wave_sum(float x) { return xrow_sum(row_sum(x)); }
__device__ __forceinline__ f32x2 fold8(const float (&v)[8]) {
  float t0 = fold32(v[0], v[4]), t1 = fold32(v[1], v[5]), t2 = fold32(v[2], v[6]), t3 = fold32(v[3], v[7]);
  f32x2 u;
  u.x = fold16(t0, t2);
  u.y = fold16(t1, t3);
  return u;
}

__device__ __forceinline__ v4u ld_nt(gcv4u* p) { return __builtin_nontemporal_load(p); }
typedef __amdgpu_buffer_rsrc_t rsrc_t;
__device__ __forceinline__ rsrc_t mk(const void* p, unsigned bytes) { return __builtin_amdgcn_make_buffer_rsrc((void*)p, 0, bytes, 0x00020000); }
template <int AUX> __device__ __forceinline__ v4u bld(rsrc_t r, unsigned voff, unsigned soff) { return __builtin_amdgcn_raw_buffer_load_b128(r, voff, soff, AUX); }
#define SB() __builtin_amdgcn_sched_barrier(0)
#define QST 16
#define QLD 16
#define FST 16
#define FLD 16
#define SST 16
#define OST 16
#define OLD 16
#define PST 16
#define PLD 16
#define PRE 0
#define SCAUX 16
constexpr int SC1 = SCAUX;
template <int AUX>
__device__ __forceinline__ void put2(rsrc_t rw, unsigned voff, unsigned soff, float a, float b, unsigned tag32, unsigned tag8) {
  const v4u g = {((__float_as_uint(a) + 8u) & 0xfffffff0u) | tag8, tag32, ((__float_as_uint(b) + 8u) & 0xfffffff0u) | tag8, tag32};
  __builtin_amdgcn_raw_buffer_store_b128(g, rw, voff, soff, AUX);
}
struct TagChk {
  unsigned b32 = 0, b8 = 0;
  __device__ __forceinline__ f32x2 take(const v4u& x, unsigned tag32, unsigned tag8) {
    b32 |= __builtin_amdgcn_bitop3_b32(x.y, x.w, tag32, 0x7e);
    const unsigned a = x.x ^ tag8, b = x.z ^ tag8;
    b8 |= a | b;
    return f32x2{__uint_as_float(a), __uint_as_float(b)};
  }
  __device__ __forceinline__ bool ok() const { return __all((b32 | (b8 & 0xfu)) == 0u); }
};
template <int N, int AUX>
__device__ __forceinline__ f32x2 gather_sum(rsrc_t rw, unsigned voff, unsigned soff, unsigned stride, unsigned tag32, unsigned tag8) {
  f32x2 sum;
#pragma nounroll
  for (unsigned spins = 0; spins < SPIN_CAP; ++spins) {
    v4u x[N];
#pragma unroll
    for (int k = 0; k < N; ++k) x[k] = bld<AUX>(rw, voff, soff + k * stride);
    SB();
    TagChk tc;
    f32x2 v[N];
#pragma unroll
    for (int k = 0; k < N; ++k) v[k] = tc.take(x[k], tag32, tag8);
    sum = v[0];
#pragma unroll
    for (int k = 1; k < N; ++k) sum += v[k];
    asm volatile("" : "+v"(sum));
    if (tc.ok()) return sum;
    __builtin_amdgcn_s_sleep(1);
  }
  return f32x2{__uint_as_float(0x7fc00000u), __uint_as_float(0x7fc00000u)};
}
#define STAMP(k)
constexpr int NTA = 2;
#define KAUX 2
#define VAUX 2
#define WAUX 2

template <int N> struct IC { static constexpr int value = N; };
struct Lds {
  unsigned q[64];
  unsigned xn[40];
  unsigned tag[4];
  unsigned xflag, qflag, mcnt, gcnt;
  float ml[NWAVE * 2];
  float acc[NWAVE * 128];
  float p[NWAVE * 128];
  float gu[10 * 128];
  unsigned o[128];
  unsigned hv[64];
};
template <int N> __device__ __forceinline__ void waitvm() { asm volatile("s_waitcnt vmcnt(%0)" ::"i"(N) : "memory"); }
template <int A, int B> __device__ __forceinline__ void waitvm2(int w) {
  asm volatile("s_cmp_eq_u32 %0, 0\n\ts_cbranch_scc1 1f\n\ts_waitcnt vmcnt(%2)\n1:\n\ts_waitcnt vmcnt(%1)" ::"s"(w), "i"(A), "i"(B) : "scc", "memory");
}
__device__ __forceinline__ void lds_wait(unsigned* word, unsigned want) {
  unsigned spins = 0;
  while (__builtin_amdgcn_readfirstlane(__hip_atomic_load((LAS unsigned*)word, RLX_WG)) != want && ++spins < (1u << 22)) __builtin_amdgcn_s_sleep(2);
  asm volatile("" ::: "memory");
}
#define WND 2
#define WND0 4
#define JX 1
#define JS 14
#define JG 4
#define JQ 10
#define NPRE 19
#define JW 99
#define WND2 3
#define JH 12
#define HW 0
#define JF 6
#define PRIO 0
constexpr int nissued(int j) { const int n = j + 1 + (j < JW ? WND : WND2); return n < NPRE ? n : NPRE; }
#define ROTM 7

__global__ void __launch_bounds__(NT, 4)
fused_decode(const unsigned short* __restrict__ x_, const unsigned short* __restrict__ kc_, const unsigned short* __restrict__ vc_,
             const unsigned short* __restrict__ wq_, const unsigned short* __restrict__ wo_, const unsigned short* __restrict__ wg_,
             const unsigned short* __restrict__ wu_, const unsigned short* __restrict__ wd_, unsigned short* __restrict__ out_,
             unsigned long long* ws_) {
  __shared__ __attribute__((aligned(16))) Lds L;
  constexpr float LOG2E = 1.4426950408889634f;
  const int b = blockIdx.x, xcd = b & 7, bi = b >> 3;
  const int h = (bi >> 3) * 8 + xcd, s = bi & 7;
  const int tid = threadIdx.x, lane = tid & 63, w = __builtin_amdgcn_readfirstlane(tid >> 6), r = lane >> 4, c = lane & 15;
  const int rr = lane >> 3;
  const bool ffn = h < NFG;
  gu64* ws = (gu64*)ws_;
  const rsrc_t rw = mk(ws_, WS_END);
  v4u cpf;
  {
    unsigned long long pc;
    unsigned clen;
    asm volatile("s_getpc_b64 %0\n.Lkcode_begin:\n\ts_mov_b32 %1, .Lkcode_end-.Lkcode_begin" : "=s"(pc), "=s"(clen));
    const rsrc_t rc = mk((const void*)pc, clen);
    cpf = bld<0>(rc, tid * 16, 0);
    SB();
  }
  asm volatile("" ::"s"(x_), "s"(kc_), "s"(vc_), "s"(wq_), "s"(wo_), "s"(wg_), "s"(wu_), "s"(wd_), "s"(out_), "s"(ws_));
  STAMP(0);
  unsigned tch = 0;
  if (w != 0) {
    const size_t kvo = ((size_t)h * SK + (SK - WIN) + s * KPW) * DH;
    const int hf = ffn ? h : 0;
    const unsigned short* tb = w == 1 ? kc_ + kvo : w == 2 ? vc_ + kvo : w == 3 ? wo_ + (size_t)(h * DH) * D + 80 * s : w == 4 ? wd_ + (size_t)(hf * 64) * D + 80 * s
                             : w == 5 ? wg_ + (size_t)(80 * s) * F + 64 * hf : w == 6 ? wu_ + (size_t)(80 * s) * F + 64 * hf : out_ + 80 * s + 16 * (h < 5 ? h : 0);
    const bool ton = w < 4 || (w < 7 && ffn) || (w == 7 && h < 5);
    const rsrc_t rt = mk(tb, __builtin_amdgcn_readfirstlane(ton ? 4 : 0));
    tch = __builtin_amdgcn_raw_buffer_load_b32(rt, lane * 4, 0, NTA);
    SB();
  }
  const rsrc_t rk = mk(kc_ + ((size_t)h * SK + (SK - WIN) + s * KPW + 32 * w) * DH, 32 * DH * 2);
  const rsrc_t rv = mk(vc_ + ((size_t)h * SK + (SK - WIN) + s * KPW + 32 * w) * DH, 32 * DH * 2);
  const rsrc_t ro = mk(wo_ + (size_t)(h * DH) * D + 80 * s, DH * D * 2 - 160 * s);
  const rsrc_t rd = mk(wd_ + (size_t)((ffn ? h : 0) * 64) * D + 80 * s, ffn ? 64 * D * 2 - 160 * s : 0);
  const unsigned ooff = c < 10 ? r * (D * 2) + 16 * c : 0x40000000u;
  const unsigned ooff2 = c < 10 ? r * (2 * D * 2) + 16 * c : 0x40000000u;
  v4u sel;
  {
    const unsigned hm = c < 8 ? ((c & 1) ? 0xffff0000u : 0x0000ffffu) : 0u;
    sel = v4u{(c >> 1) == 0 ? hm : 0u, (c >> 1) == 1 ? hm : 0u, (c >> 1) == 2 ? hm : 0u, (c >> 1) == 3 ? hm : 0u};
  }
  const int rho = 4 * w + r;
  const int rot = (int)(((unsigned)(b * 16 + w) * 2654435761u) >> 16) & ROTM;
  v4u ld[19];
  auto issue = [&](int i) __attribute__((always_inline)) {
    if (i < 8) ld[i] = bld<KAUX>(rk, lane * 16, ((i + rot) & 7) * 1024);
    else if (i < 16) ld[i] = bld<VAUX>(rv, lane * 16, ((i + rot) & 7) * 1024);
    else if (i == 16) ld[i] = bld<WAUX>(ro, ooff2, w * (8 * D * 2));
    else if (i == 17) ld[i] = bld<WAUX>(ro, ooff2, w * (8 * D * 2) + D * 2);
    else if (i == 18) ld[i] = bld<WAUX>(rd, ooff, w * (4 * D * 2));
    SB();
  };
  v4u gv, uv;
  if (w == 0) {
    __builtin_amdgcn_s_setprio(3);
    const rsrc_t rx = mk(x_, D * 2);
    const v4u a4 = bld<0>(rx, 20 * lane, 0);
    const unsigned a1 = __builtin_amdgcn_raw_buffer_load_b32(rx, 20 * lane + 16, 0, 0);
    const v4u b4 = bld<0>(rx, 20 * rr, 160 * s);
    const unsigned b1 = __builtin_amdgcn_raw_buffer_load_b32(rx, 20 * rr + 16, 160 * s, 0);
    const unsigned long long ep = __hip_atomic_load(ws, RLX_AGENT);
    SB();
    constexpr int QS = HQ * DH * 2;
    const rsrc_t rq = mk(wq_ + (size_t)(80 * s) * (HQ * DH) + h * DH, 79 * QS + 256);
    const unsigned woff = rr * 10 * QS + 16 * (lane & 7);
    v4u wv[20];
#pragma unroll
    for (int i = 0; i < 20; ++i) { wv[i] = bld<WAUX>(rq, woff + (i & 1) * 128, (i >> 1) * QS); SB(); }
    if (lane == 0) { L.xflag = 0u; L.qflag = 0u; L.mcnt = 0u; L.gcnt = 0u; }
    __syncthreads();
    SB();
    const unsigned e_lo = __builtin_amdgcn_readfirstlane((unsigned)ep), e_hi = __builtin_amdgcn_readfirstlane((unsigned)(ep >> 32));
    unsigned tag32 = (e_lo + 1u) * 0x9E3779B1u; tag32 ^= tag32 >> 15; tag32 *= 0x85EBCA77u; tag32 ^= tag32 >> 13; tag32 ^= 0x5bd1e995u;
    const unsigned tag8 = (((e_hi ^ (e_lo * 0xC2B2AE35u)) * 0x27D4EB2Fu) >> 28) ^ 0xAu;
    float xs[10];
#pragma unroll
    for (int i = 0; i < 4; ++i) { xs[2 * i] = bf_lo(a4[i]); xs[2 * i + 1] = bf_hi(a4[i]); }
    xs[8] = bf_lo(a1); xs[9] = bf_hi(a1);
    float sum = 0.f;
#pragma unroll
    for (int i = 0; i < 10; ++i) sum += xs[i];
    const float mean = wave_sum(sum) * (1.f / D);
    float sq = 0.f;
#pragma unroll
    for (int i = 0; i < 10; ++i) { const float d = xs[i] - mean; sq = fmaf(d, d, sq); }
    const float var = wave_sum(sq) * (1.f / D);
    const float mean_b = rbf(mean);
    const float den = rbf(sqrtf(rbf(rbf(var) + 1e-5f)));
    const unsigned xb[5] = {b4[0], b4[1], b4[2], b4[3], b1};
    unsigned xp[5];
#pragma unroll
    for (int i = 0; i < 5; ++i) xp[i] = cvtpk(rbf(bf_lo(xb[i]) - mean_b) / den, rbf(bf_hi(xb[i]) - mean_b) / den);
    if ((lane & 7) == 0) {
#pragma unroll
      for (int i = 0; i < 5; ++i) L.xn[5 * rr + i] = xp[i];
    }
    if (lane == 0) { L.tag[0] = tag32; L.tag[1] = tag8; L.tag[2] = e_lo; L.tag[3] = e_hi; }
    asm volatile("s_waitcnt lgkmcnt(0)" ::: "memory");
    if (lane == 0) __hip_atomic_store((LAS unsigned*)&L.xflag, 1u, RLX_WG);
    float accA[8], accB[8];
#pragma unroll
    for (int k = 0; k < 8; ++k) { accA[k] = 0.f; accB[k] = 0.f; }
#pragma unroll
    for (int i = 0; i < 10; ++i) {
      const unsigned xlo = (i & 1) ? xp[i >> 1] >> 16 : xp[i >> 1] & 0xffffu, xhi = (i & 1) ? xp[i >> 1] & 0xffff0000u : xp[i >> 1] << 16;
#pragma unroll
      for (int k = 0; k < 4; ++k) {
        accA[2 * k] = dot2(wv[2 * i][k], xlo, accA[2 * k]);
        accA[2 * k + 1] = dot2(wv[2 * i][k], xhi, accA[2 * k + 1]);
        accB[2 * k] = dot2(wv[2 * i + 1][k], xlo, accB[2 * k]);
        accB[2 * k + 1] = dot2(wv[2 * i + 1][k], xhi, accB[2 * k + 1]);
      }
    }
    f32x2 ua = fold8(accA), ub = fold8(accB);
    ua.x += dpp<ROR8>(ua.x); ua.y += dpp<ROR8>(ua.y); ub.x += dpp<ROR8>(ub.x); ub.y += dpp<ROR8>(ub.y);
    const f32x2 u = c < 8 ? ua : ub;
    put2<QST>(rw, lane * 16, WS_QG + (h * NSPLIT + s) * 1024, u.x, u.y, tag32, tag8);
    STAMP(1);
  } else {
    __syncthreads();
    if (PRIO == 1) { if (w >= 12) __builtin_amdgcn_s_setprio(3); else if (w >= 8) __builtin_amdgcn_s_setprio(2); else if (w >= 4) __builtin_amdgcn_s_setprio(1); }
    if (PRIO == 2) { if (w >= 8) __builtin_amdgcn_s_setprio(1); }
  }
  {
    const unsigned gbytes = __builtin_amdgcn_readfirstlane((ffn && w >= 1 && w <= 10) ? 7 * F * 2 + 128 : 0);
    const size_t goff = (size_t)(80 * s + 8 * ((w + 15) & 15)) * F + 64 * (ffn ? h : 0);
    const bool gon = w >= 1 && w <= 10;
    const rsrc_t rg = mk(wg_ + (gon ? goff : 0), gbytes), ru = mk(wu_ + (gon ? goff : 0), gbytes);
    SB();
    gv = bld<WAUX>(rg, rr * (F * 2) + 16 * (lane & 7), 0);
    uv = bld<WAUX>(ru, rr * (F * 2) + 16 * (lane & 7), 0);
    SB();
  }
  v4u qp;
  unsigned tag32 = 0, tag8 = 0;
  float sc[8], m_w = 0.f;
  unsigned pk[8];
  f32x4 acc = {0.f, 0.f, 0.f, 0.f};
  auto qk = [&](int i) __attribute__((always_inline)) {
    constexpr float SCALE = 0.08838834764831845f;
    float d = 0.f;
#pragma unroll
    for (int k = 0; k < 4; ++k) d = dot2(ld[i][k], qp[k], d);
    sc[i] = rbf(rbf(row_sum(d)) * SCALE);
  };
  auto pv = [&](int i) __attribute__((always_inline)) {
    acc = mfma_rows(pk[i], sel, ld[8 + i], acc);
    asm volatile("" : "+v"(acc));
  };
#pragma unroll
  for (int i = 0; i < WND; ++i) issue(i);
  auto step = [&](auto J) __attribute__((always_inline)) {
    constexpr int j = decltype(J)::value;
    if constexpr (nissued(j) > nissued(j - 1)) {
      waitvm2<nissued(j - 1) - j - 1 + WND0 - WND, nissued(j - 1) - j - 1>(w);
#pragma unroll
      for (int i = nissued(j - 1); i < nissued(j); ++i) issue(i);
    }
    if constexpr (j == JX) if (w == 0) {
      const unsigned t32 = __builtin_amdgcn_readfirstlane(L.tag[0]), t8 = __builtin_amdgcn_readfirstlane(L.tag[1]);
      const f32x2 qs = gather_sum<8, QLD>(rw, lane * 16, WS_QG + h * NSPLIT * 1024, 1024, t32, t8);
      const float q0 = qs.x, q1 = qs.y;
      L.q[4 * c + r] = cvtpk(q0, q1);
      asm volatile("s_waitcnt lgkmcnt(0)" ::: "memory");
      if (lane == 0) __hip_atomic_store((LAS unsigned*)&L.qflag, 1u, RLX_WG);
      STAMP(8);
      SB();
    }
    if constexpr (j == JG) {
      asm volatile("" ::"v"(cpf));
      asm volatile("" ::"v"(tch));
      lds_wait(&L.xflag, 1u);
      tag32 = __builtin_amdgcn_readfirstlane(L.tag[0]); tag8 = __builtin_amdgcn_readfirstlane(L.tag[1]);
      if (ffn && w >= 1 && w <= 10) {
        const unsigned xw = L.xn[4 * (w - 1) + (rr >> 1)];
        const float xn = (rr & 1) ? bf_hi(xw) : bf_lo(xw);
        float g[8], u[8];
#pragma unroll
        for (int k = 0; k < 4; ++k) {
          g[2 * k] = xn * bf_lo(gv[k]); g[2 * k + 1] = xn * bf_hi(gv[k]);
          u[2 * k] = xn * bf_lo(uv[k]); u[2 * k + 1] = xn * bf_hi(uv[k]);
        }
        f32x2 gs = fold8(g), us = fold8(u);
        gs.x += dpp<ROR8>(gs.x); gs.y += dpp<ROR8>(gs.y); us.x += dpp<ROR8>(us.x); us.y += dpp<ROR8>(us.y);
        *(f32x2*)(L.gu + (w - 1) * 128 + 8 * c + 2 * r) = c < 8 ? gs : us;
        asm volatile("s_waitcnt lgkmcnt(0)" ::: "memory");
        if (lane == 0) __hip_atomic_fetch_add((LAS unsigned*)&L.gcnt, 1u, RLX_WG);
      }
      SB();
    }
    if constexpr (j == JF) if (w == 0 && ffn) {
      lds_wait(&L.gcnt, 10u);
      float gg = 0.f, uu = 0.f;
#pragma unroll
      for (int t = 0; t < 10; ++t) { gg += L.gu[t * 128 + lane]; uu += L.gu[t * 128 + 64 + lane]; }
      put2<FST>(rw, lane * 16, WS_FG + (h * NSPLIT + s) * 1024, gg, uu, tag32, tag8);
      SB();
    }
    if constexpr (j == JQ) {
      STAMP(2);
      lds_wait(&L.qflag, 1u);
      qp = *(const v4u*)(L.q + 4 * c);
#pragma unroll
      for (int i = 0; i < 4; ++i) qk(i);
      SB();
    }
    if constexpr (j == JQ + 1) {
#pragma unroll
      for (int i = 4; i < 8; ++i) qk(i);
      float m = sc[0];
#pragma unroll
      for (int i = 1; i < 8; ++i) m = fmaxf(m, sc[i]);
      m_w = xrow_max(m);
      float l = 0.f;
#pragma unroll
      for (int i = 0; i < 8; ++i) { const float pe = __builtin_amdgcn_exp2f((sc[i] - m_w) * LOG2E); l += pe; pk[i] = cvtpk(pe, pe); }
      const float l_w = xrow_sum(l);
      if (lane == 0) {
        *(f32x2*)(L.ml + 2 * w) = f32x2{m_w, l_w};
        asm volatile("s_waitcnt lgkmcnt(0)" ::: "memory");
        __hip_atomic_fetch_add((LAS unsigned*)&L.mcnt, 1u, RLX_WG);
      }
      STAMP(3);
      SB();
    }
    if constexpr (j == JQ + 2) {
#pragma unroll
      for (int i = 0; i <= j - 8; ++i) pv(i);
      SB();
    }
    if constexpr (j > JQ + 2) { pv(j - 8); SB(); }
    if constexpr (j == JH) if (w == HW) {
      float hv = 0.f;
      if (ffn) {
        const f32x2 gu = gather_sum<8, FLD>(rw, lane * 16, WS_FG + h * NSPLIT * 1024, 1024, tag32, tag8);
        float g = gu.x, u = gu.y;
        g = rbf(g); u = rbf(u);
        const float sg = rbf(1.f / (1.f + __builtin_amdgcn_exp2f(-g * LOG2E)));
        hv = rbf(sg * u);
      }
      L.hv[lane] = cvtpk(hv, hv);
      STAMP(9);
    }
  };
  step(IC<0>{});  step(IC<1>{});  step(IC<2>{});  step(IC<3>{});
  step(IC<4>{});  step(IC<5>{});  step(IC<6>{});  step(IC<7>{});
  step(IC<8>{});  step(IC<9>{});  step(IC<10>{});  step(IC<11>{});
  step(IC<12>{});  step(IC<13>{});  step(IC<14>{});  step(IC<15>{});
#pragma unroll
  for (int i = nissued(15); i < NPRE; ++i) issue(i);
  if (lane < 32) *(f32x4*)(L.acc + w * 128 + 8 * c + 4 * r) = acc;
  STAMP(4);
  if (w == 0) {
    lds_wait(&L.mcnt, (unsigned)NWAVE);
    const f32x2 ml = *(const f32x2*)(L.ml + 2 * c);
    const float m8 = max8(ml.x), M = fmaxf(m8, dpp<ROR8>(m8));
    const float fl = __builtin_amdgcn_exp2f((ml.x - M) * LOG2E);
    const float Ls = row_sum(fl * ml.y);
    put2<SST>(rw, lane == 0 ? 0u : 0x08000000u, WS_SG + h * 128 + s * 16, M, Ls, tag32, tag8);
    float fw[NWAVE];
#pragma unroll
    for (int t = 0; t < NWAVE; ++t) fw[t] = __builtin_bit_cast(float, __builtin_amdgcn_readlane(__builtin_bit_cast(int, fl), t));
    STAMP(11);
    SB();
    __syncthreads();
    SB();
    f32x2 a[NWAVE];
#pragma unroll
    for (int t = 0; t < NWAVE; ++t) a[t] = *(const f32x2*)(L.acc + t * 128 + 2 * lane);
    f32x2 oa = a[0] * fw[0], ob = a[1] * fw[1];
#pragma unroll
    for (int t = 2; t < NWAVE; t += 2) { oa += a[t] * fw[t]; ob += a[t + 1] * fw[t + 1]; }
    const float o0 = oa.x + ob.x, o1 = oa.y + ob.y;
    put2<OST>(rw, lane * 16, WS_OG + (h * NSPLIT + s) * 1024, o0, o1, tag32, tag8);
    SB();
#pragma unroll
    for (int i = NPRE; i < 19; ++i) issue(i);
    STAMP(5);
    f32x2 o2 = {__uint_as_float(0x7fc00000u), __uint_as_float(0x7fc00000u)};
#pragma nounroll
    for (unsigned spins = 0; spins < SPIN_CAP; ++spins) {
      const v4u xs = bld<OLD>(rw, (lane & 7) * 16, WS_SG + h * 128);
      v4u x[8];
#pragma unroll
      for (int k = 0; k < 8; ++k) x[k] = bld<OLD>(rw, lane * 16, WS_OG + (h * NSPLIT + k) * 1024);
      SB();
      TagChk tc;
      const f32x2 st = tc.take(xs, tag32, tag8);
      const float MM = max8(st.x);
      const float fs = __builtin_amdgcn_exp2f((st.x - MM) * LOG2E);
      const float ws8 = fs * __builtin_amdgcn_rcpf(sum8(fs * st.y));
      float wk[8];
#pragma unroll
      for (int k = 0; k < 8; ++k) wk[k] = __builtin_bit_cast(float, __builtin_amdgcn_readlane(__builtin_bit_cast(int, ws8), k));
      SB();
      f32x2 acc2 = tc.take(x[0], tag32, tag8) * wk[0];
#pragma unroll
      for (int k = 1; k < 8; ++k) acc2 += tc.take(x[k], tag32, tag8) * wk[k];
      asm volatile("" : "+v"(acc2));
      if (tc.ok()) { o2 = acc2; break; }
      __builtin_amdgcn_s_sleep(1);
    }
    STAMP(10);
    *(v2u*)(L.o + 2 * lane) = v2u{cvtpk(o2.x, o2.x), cvtpk(o2.y, o2.y)};
  } else {
    __syncthreads();
    SB();
#pragma unroll
    for (int i = NPRE; i < 19; ++i) issue(i);
    STAMP(5);
  }
  f32x4 pa = mfma_rows(L.hv[rho], sel, ld[18], f32x4{0.f, 0.f, 0.f, 0.f});
  asm volatile("" : "+v"(pa));
  SB();
  __syncthreads();
  SB();
  {
    const v2u oc = *(const v2u*)(L.o + 2 * rho);
    pa = mfma_rows(oc.x, sel, ld[16], pa);
    const f32x4 pb = mfma_rows(oc.y, sel, ld[17], f32x4{0.f, 0.f, 0.f, 0.f});
    if (lane < 32) *(f32x4*)(L.p + w * 128 + 8 * c + 4 * r) = pa + pb;
  }
  __syncthreads();
  STAMP(6);
  if (tid < 40) {
    f32x2 a[NWAVE];
#pragma unroll
    for (int t = 0; t < NWAVE; ++t) a[t] = *(const f32x2*)(L.p + t * 128 + 2 * tid);
#pragma unroll
    for (int st = 1; st < NWAVE; st *= 2)
#pragma unroll
      for (int t = 0; t < NWAVE; t += 2 * st) a[t] += a[t + st];
    put2<PST>(rw, (tid >> 3) * 4096 + (tid & 7) * 16, WS_PG + s * 5 * 4096 + h * 128, a[0].x, a[0].y, tag32, tag8);
  }
#define NPOLL 1
#define PSTEP 4
  if (w >= 2 && w < 2 + NPOLL && h < 5) {
    if (NPOLL > 1) { if (w == 3) __builtin_amdgcn_s_sleep(PSTEP); else if (w == 4) __builtin_amdgcn_s_sleep(2 * PSTEP); else if (w == 5) __builtin_amdgcn_s_sleep(3 * PSTEP); }
    const int col = 80 * s + 16 * h + 2 * (lane & 7);
    const unsigned xv = *(gcu32*)((gcu16*)x_ + col);
    const unsigned long long enext = (((unsigned long long)L.tag[3] << 32) | L.tag[2]) + 1ull;
    auto finish = [&](f32x2 a) __attribute__((always_inline)) {
      a.x += dpp<ROR8>(a.x); a.y += dpp<ROR8>(a.y);
      a.x = fold16(a.x, a.y);
      a.x = fold32(a.x, a.x);
      const float xr = r == 0 ? bf_lo(xv) : bf_hi(xv);
      if (lane < 32 && c < 8) ((GAS unsigned short*)out_)[col + r] = (unsigned short)cvtpk(xr + a.x, 0.f);
      if (b == 0 && lane == 0) __hip_atomic_store(ws, enext, RLX_AGENT);
    };
    const f32x2 fsum = gather_sum<4, PLD>(rw, lane * 16, WS_PG + (s * 5 + h) * 4096, 1024, tag32, tag8);
    if (NPOLL > 1) {
      unsigned old = 0;
      if (lane == 0) old = __hip_atomic_fetch_add((LAS unsigned*)&L.xflag, 1u, RLX_WG);
      if (__builtin_amdgcn_readfirstlane(old) == 1u) finish(fsum);
    } else finish(fsum);
  }
  asm volatile(".Lkcode_end:" ::: "memory");
}

extern "C" void kernel_launch(void* const* d_in, const int* in_sizes, int n_in, void* d_out, int out_size, void* d_ws,
                              size_t ws_size, hipStream_t stream) {
  (void)in_sizes; (void)n_in; (void)out_size; (void)ws_size;
  hipLaunchKernelGGL(fused_decode, dim3(256), dim3(NT), 0, stream, (const unsigned short*)d_in[0], (const unsigned short*)d_in[1],
                     (const unsigned short*)d_in[2], (const unsigned short*)d_in[3], (const unsigned short*)d_in[4],
                     (const unsigned short*)d_in[6], (const unsigned short*)d_in[7], (const unsigned short*)d_in[8],
                     (unsigned short*)d_out, (unsigned long long*)((char*)d_ws + WSOFF));
}
```

```cpp
#include <hip/hip_runtime.h>
#include <hip/hip_bf16.h>
#include <stdint.h>

#define GAS __attribute__((address_space(1)))
#define LAS __attribute__((address_space(3)))
typedef unsigned v4u __attribute__((ext_vector_type(4)));
typedef unsigned v2u __attribute__((ext_vector_type(2)));
typedef float f32x2 __attribute__((ext_vector_type(2)));
typedef float f32x4 __attribute__((ext_vector_type(4)));
typedef __attribute__((ext_vector_type(2))) __bf16 bf16x2;
typedef __attribute__((ext_vector_type(8))) __bf16 bf16x8;
typedef GAS unsigned long long gu64;
typedef __amdgpu_buffer_rsrc_t rsrc_t;
#define RLX_AGENT __ATOMIC_RELAXED, __HIP_MEMORY_SCOPE_AGENT
#define RLX_WG __ATOMIC_RELAXED, __HIP_MEMORY_SCOPE_WORKGROUP
#define SB() __builtin_amdgcn_sched_barrier(0)
#define INL __attribute__((always_inline))

constexpr int D = 640, HQ = 32, DH = 128, SK = 32768, WIN = 4096, F = 1728;
constexpr int NSPLIT = 8, KPW = WIN / NSPLIT;
constexpr int NT = 1024, NWAVE = 16;
constexpr int NFG = F / 64;
constexpr unsigned SPIN_CAP = 1u << 16;
constexpr int NTA = 2, SC1 = 16;
constexpr unsigned OOB = 0x08000000u;

constexpr unsigned WS_QG = 1024;
constexpr unsigned WS_FG = WS_QG + HQ * NSPLIT * 1024;
constexpr unsigned WS_OG = WS_FG + HQ * NSPLIT * 1024;
constexpr unsigned WS_SG = WS_OG + HQ * NSPLIT * 1024;
constexpr unsigned WS_PG = WS_SG + HQ * 128;
constexpr unsigned WS_END = WS_PG + 8 * 5 * 32 * 128;

constexpr int WND = 4;
constexpr int JG = 1;
constexpr int JX = 3;
constexpr int JF = 4;
constexpr int JQ = 5;
constexpr int NLD = 19;
constexpr int PACE = 63, PACE0 = 120, PACEW = 30, FG_TICK = 800;
constexpr int due(int i) { return PACE0 + (i <= 15 ? i * PACE : 15 * PACE + (i - 15) * PACEW); }
constexpr int ODELAY = 16, PDELAY = 24;

__device__ __forceinline__ float bf_lo(unsigned w) { return __builtin_bit_cast(float, w << 16); }
__device__ __forceinline__ float bf_hi(unsigned w) { return __builtin_bit_cast(float, w & 0xffff0000u); }
__device__ __forceinline__ unsigned cvtpk(float lo, float hi) {
  unsigned r;
  asm("v_cvt_pk_bf16_f32 %0, %1, %2" : "=v"(r) : "v"(lo), "v"(hi));
  return r;
}
__device__ __forceinline__ float rbf(float x) { return bf_lo(cvtpk(x, x)); }
__device__ __forceinline__ float dot2(unsigned a, unsigned b, float acc) {
  return __builtin_amdgcn_fdot2_f32_bf16(__builtin_bit_cast(bf16x2, a), __builtin_bit_cast(bf16x2, b), acc, false);
}
__device__ __forceinline__ f32x4 mfma_rows(unsigned pk, const v4u& sel, const v4u& wreg, f32x4 acc) {
  const v4u a = {pk & sel.x, pk & sel.y, pk & sel.z, pk & sel.w};
  return __builtin_amdgcn_mfma_f32_16x16x32_bf16(__builtin_bit_cast(bf16x8, a), __builtin_bit_cast(bf16x8, wreg), acc, 0, 0, 0);
}
template <int CTRL> __device__ __forceinline__ float dpp(float x) {
  return __builtin_bit_cast(float, __builtin_amdgcn_mov_dpp(__builtin_bit_cast(int, x), CTRL, 0xf, 0xf, true));
}
constexpr int XOR1 = 0xB1, XOR2 = 0x4E, HMIR = 0x141, ROR8 = 0x128;
__device__ __forceinline__ float sum8(float x) { x += dpp<XOR1>(x); x += dpp<XOR2>(x); x += dpp<HMIR>(x); return x; }
__device__ __forceinline__ float max8(float x) { x = fmaxf(x, dpp<XOR1>(x)); x = fmaxf(x, dpp<XOR2>(x)); x = fmaxf(x, dpp<HMIR>(x)); return x; }
__device__ __forceinline__ float row_sum(float x) { x = sum8(x); x += dpp<ROR8>(x); return x; }
__device__ __forceinline__ float fold32(float a, float b) {
  auto t = __builtin_amdgcn_permlane32_swap(__float_as_uint(a), __float_as_uint(b), false, false);
  return __uint_as_float(t[0]) + __uint_as_float(t[1]);
}
__device__ __forceinline__ float fold16(float a, float b) {
  auto t = __builtin_amdgcn_permlane16_swap(__float_as_uint(a), __float_as_uint(b), false, false);
  return __uint_as_float(t[0]) + __uint_as_float(t[1]);
}
__device__ __forceinline__ float xrow_sum(float x) { x = fold16(x, x); return fold32(x, x); }
__device__ __forceinline__ float xrow_max(float x) {
  auto s = __builtin_amdgcn_permlane16_swap(__float_as_uint(x), __float_as_uint(x), false, false);
  x = fmaxf(__uint_as_float(s[0]), __uint_as_float(s[1]));
  auto t = __builtin_amdgcn_permlane32_swap(__float_as_uint(x), __float_as_uint(x), false, false);
  return fmaxf(__uint_as_float(t[0]), __uint_as_float(t[1]));
}
__device__ __forceinline__ float wave_sum(float x) { return xrow_sum(row_sum(x)); }
__device__ __forceinline__ f32x2 fold8(const float (&v)[8]) {
  float t0 = fold32(v[0], v[4]), t1 = fold32(v[1], v[5]), t2 = fold32(v[2], v[6]), t3 = fold32(v[3], v[7]);
  return f32x2{fold16(t0, t2), fold16(t1, t3)};
}
__device__ __forceinline__ float rdlane(float x, int l) { return __builtin_bit_cast(float, __builtin_amdgcn_readlane(__builtin_bit_cast(int, x), l)); }

__device__ __forceinline__ rsrc_t mk(const void* p, unsigned bytes) { return __builtin_amdgcn_make_buffer_rsrc((void*)p, 0, bytes, 0x00020000); }
template <int AUX> __device__ __forceinline__ v4u bld(rsrc_t r, unsigned voff, unsigned soff) { return __builtin_amdgcn_raw_buffer_load_b128(r, voff, soff, AUX); }

__device__ __forceinline__ void put2(rsrc_t rw, unsigned voff, unsigned soff, float a, float b, unsigned tag32, unsigned tag4) {
  const v4u g = {((__float_as_uint(a) + 8u) & 0xfffffff0u) | tag4, tag32, ((__float_as_uint(b) + 8u) & 0xfffffff0u) | tag4, tag32};
  __builtin_amdgcn_raw_buffer_store_b128(g, rw, voff, soff, SC1);
}
struct TagChk {
  unsigned b32 = 0, b4 = 0;
  __device__ __forceinline__ f32x2 take(const v4u& x, unsigned tag32, unsigned tag4) {
    b32 |= __builtin_amdgcn_bitop3_b32(x.y, x.w, tag32, 0x7e);
    const unsigned a = x.x ^ tag4, b = x.z ^ tag4;
    b4 |= a | b;
    return f32x2{__uint_as_float(a), __uint_as_float(b)};
  }
  __device__ __forceinline__ bool ok() const { return __all((b32 | (b4 & 0xfu)) == 0u); }
};
template <int N>
__device__ __forceinline__ f32x2 gather_sum(rsrc_t rw, unsigned voff, unsigned soff, unsigned tag32, unsigned tag4) {
#pragma nounroll
  for (unsigned spins = 0; spins < SPIN_CAP; ++spins) {
    asm volatile("" ::: "memory");
    v4u x[N];
#pragma unroll
    for (int k = 0; k < N; ++k) x[k] = bld<SC1>(rw, voff, soff + k * 1024);
    SB();
    TagChk tc;
    f32x2 v[N];
#pragma unroll
    for (int k = 0; k < N; ++k) v[k] = tc.take(x[k], tag32, tag4);
    f32x2 sum = v[0];
#pragma unroll
    for (int k = 1; k < N; ++k) sum += v[k];
    asm volatile("" : "+v"(sum));
    if (tc.ok()) return sum;
    __builtin_amdgcn_s_sleep(1);
  }
  return f32x2{__uint_as_float(0x7fc00000u), __uint_as_float(0x7fc00000u)};
}

template <int N> struct IC { static constexpr int value = N; };
struct Lds {
  unsigned q[64];
  unsigned xn[40];
  unsigned tag[4];
  unsigned xflag, qflag, mcnt, gcnt;
  float ml[NWAVE * 2];
  float acc[NWAVE * 128];
  float p[NWAVE * 128];
  float gu[10 * 128];
  unsigned o[128];
  unsigned hv[64];
};
template <int N> __device__ __forceinline__ void waitvm_not0(int w) {
  asm volatile("s_cmp_eq_u32 %0, 0\n\ts_cbranch_scc1 1f\n\ts_waitcnt vmcnt(%1)\n1:" ::"s"(w), "i"(N) : "scc", "memory");
}
__device__ __forceinline__ unsigned now32() {
  unsigned long long t;
  asm volatile("s_memrealtime %0\n\ts_waitcnt lgkmcnt(0)" : "=s"(t)::"memory");
  return (unsigned)t;
}
__device__ __forceinline__ void wait_until(unsigned t) {
#pragma nounroll
  while ((int)(now32() - t) < 0) __builtin_amdgcn_s_sleep(1);
}
__device__ __forceinline__ void lds_wait(unsigned* word, unsigned want) {
  unsigned spins = 0;
  while (__builtin_amdgcn_readfirstlane(__hip_atomic_load((LAS unsigned*)word, RLX_WG)) != want && ++spins < (1u << 22)) __builtin_amdgcn_s_sleep(2);
  asm volatile("" ::: "memory");
}
#define LDS_DRAIN() asm volatile("s_waitcnt lgkmcnt(0)" ::: "memory")

__global__ void __launch_bounds__(NT, 4)
fused_decode(const unsigned short* __restrict__ x_, const unsigned short* __restrict__ kc_, const unsigned short* __restrict__ vc_,
             const unsigned short* __restrict__ wq_, const unsigned short* __restrict__ wo_, const unsigned short* __restrict__ wg_,
             const unsigned short* __restrict__ wu_, const unsigned short* __restrict__ wd_, unsigned short* __restrict__ out_,
             unsigned long long* ws_) {
  __shared__ __attribute__((aligned(16))) Lds L;
  constexpr float LOG2E = 1.4426950408889634f;
  const int b = blockIdx.x, bi = b >> 3;
  const int h = (bi >> 3) * 8 + (b & 7), s = bi & 7;
  const int tid = threadIdx.x, lane = tid & 63, w = __builtin_amdgcn_readfirstlane(tid >> 6), r = lane >> 4, c = lane & 15;
  const int rr = lane >> 3;
  const bool ffn = h < NFG;
  gu64* ws = (gu64*)ws_;
  const rsrc_t rw = mk(ws_, WS_END);
  v4u cpf;
  {
    unsigned long long pc;
    unsigned clen;
    asm volatile("s_getpc_b64 %0\n.Lkcode_begin:\n\ts_mov_b32 %1, .Lkcode_end-.Lkcode_begin" : "=s"(pc), "=s"(clen));
    cpf = bld<0>(mk((const void*)pc, clen), tid * 16, 0);
    SB();
  }
  asm volatile("" ::"s"(x_), "s"(kc_), "s"(vc_), "s"(wq_), "s"(wo_), "s"(wg_), "s"(wu_), "s"(wd_), "s"(out_), "s"(ws_));
  const unsigned tstart = now32();
  const unsigned tbase = tstart + (unsigned)(w * PACE / 16) - (w == 0 ? 100000u : 0u);
  const rsrc_t rk = mk(kc_ + ((size_t)h * SK + (SK - WIN) + s * KPW + 32 * w) * DH, 32 * DH * 2);
  const rsrc_t rv = mk(vc_ + ((size_t)h * SK + (SK - WIN) + s * KPW + 32 * w) * DH, 32 * DH * 2);
  const rsrc_t ro = mk(wo_ + (size_t)(h * DH) * D + 80 * s, DH * D * 2 - 160 * s);
  const rsrc_t rd = mk(wd_ + (size_t)((ffn ? h : 0) * 64) * D + 80 * s, ffn ? 64 * D * 2 - 160 * s : 0);
  const unsigned ooff = c < 10 ? r * (D * 2) + 16 * c : OOB;
  const unsigned ooff2 = c < 10 ? r * (2 * D * 2) + 16 * c : OOB;
  v4u sel;
  {
    const unsigned hm = c < 8 ? ((c & 1) ? 0xffff0000u : 0x0000ffffu) : 0u;
    sel = v4u{(c >> 1) == 0 ? hm : 0u, (c >> 1) == 1 ? hm : 0u, (c >> 1) == 2 ? hm : 0u, (c >> 1) == 3 ? hm : 0u};
  }
  const int rho = 4 * w + r;
  const int rot = (int)(((unsigned)(b * 16 + w) * 2654435761u) >> 16) & 7;
  v4u ld[NLD];
  auto issue = [&](int i) INL {
    if (i >= 2) wait_until(tbase + (unsigned)due(i));
    if (i < 8) ld[i] = bld<NTA>(rk, lane * 16, ((i + rot) & 7) * 1024);
    else if (i < 16) ld[i] = bld<NTA>(rv, lane * 16, ((i + rot) & 7) * 1024);
    else if (i == 16) ld[i] = bld<NTA>(ro, ooff2, w * (8 * D * 2));
    else if (i == 17) ld[i] = bld<NTA>(ro, ooff2, w * (8 * D * 2) + D * 2);
    else ld[i] = bld<NTA>(rd, ooff, w * (4 * D * 2));
    SB();
  };
  if (w == 0) {
    __builtin_amdgcn_s_setprio(3);
    const rsrc_t rx = mk(x_, D * 2);
    const v4u a4 = bld<0>(rx, 20 * lane, 0);
    const unsigned a1 = __builtin_amdgcn_raw_buffer_load_b32(rx, 20 * lane + 16, 0, 0);
    const v4u b4 = bld<0>(rx, 20 * rr, 160 * s);
    const unsigned b1 = __builtin_amdgcn_raw_buffer_load_b32(rx, 20 * rr + 16, 160 * s, 0);
    const unsigned long long ep = __hip_atomic_load(ws, RLX_AGENT);
    SB();
    constexpr int QS = HQ * DH * 2;
    const rsrc_t rq = mk(wq_ + (size_t)(80 * s) * (HQ * DH) + h * DH, 79 * QS + 256);
    const unsigned woff = rr * 10 * QS + 16 * (lane & 7);
    v4u wv[20];
#pragma unroll
    for (int i = 0; i < 20; ++i) { wv[i] = bld<NTA>(rq, woff + (i & 1) * 128, (i >> 1) * QS); SB(); }
    if (lane == 0) { L.xflag = 0u; L.qflag = 0u; L.mcnt = 0u; L.gcnt = 0u; }
    __syncthreads();
    SB();
    const unsigned e_lo = __builtin_amdgcn_readfirstlane((unsigned)ep), e_hi = __builtin_amdgcn_readfirstlane((unsigned)(ep >> 32));
    unsigned tag32 = (e_lo + 1u) * 0x9E3779B1u; tag32 ^= tag32 >> 15; tag32 *= 0x85EBCA77u; tag32 ^= tag32 >> 13; tag32 ^= 0x5bd1e995u;
    const unsigned tag4 = (((e_hi ^ (e_lo * 0xC2B2AE35u)) * 0x27D4EB2Fu) >> 28) ^ 0xAu;
    float xs[10];
#pragma unroll
    for (int i = 0; i < 4; ++i) { xs[2 * i] = bf_lo(a4[i]); xs[2 * i + 1] = bf_hi(a4[i]); }
    xs[8] = bf_lo(a1); xs[9] = bf_hi(a1);
    float sum = 0.f;
#pragma unroll
    for (int i = 0; i < 10; ++i) sum += xs[i];
    const float mean = wave_sum(sum) * (1.f / D);
    float sq = 0.f;
#pragma unroll
    for (int i = 0; i < 10; ++i) { const float d = xs[i] - mean; sq = fmaf(d, d, sq); }
    const float var = wave_sum(sq) * (1.f / D);
    const float mean_b = rbf(mean);
    const float den = rbf(sqrtf(rbf(rbf(var) + 1e-5f)));
    const unsigned xb[5] = {b4[0], b4[1], b4[2], b4[3], b1};
    unsigned xp[5];
#pragma unroll
    for (int i = 0; i < 5; ++i) xp[i] = cvtpk(rbf(bf_lo(xb[i]) - mean_b) / den, rbf(bf_hi(xb[i]) - mean_b) / den);
    if ((lane & 7) == 0) {
#pragma unroll
      for (int i = 0; i < 5; ++i) L.xn[5 * rr + i] = xp[i];
    }
    if (lane == 0) { L.tag[0] = tag32; L.tag[1] = tag4; L.tag[2] = e_lo; L.tag[3] = e_hi; }
    LDS_DRAIN();
    if (lane == 0) __hip_atomic_store((LAS unsigned*)&L.xflag, 1u, RLX_WG);
    float accA[8], accB[8];
#pragma unroll
    for (int k = 0; k < 8; ++k) { accA[k] = 0.f; accB[k] = 0.f; }
#pragma unroll
    for (int i = 0; i < 10; ++i) {
      const unsigned xlo = (i & 1) ? xp[i >> 1] >> 16 : xp[i >> 1] & 0xffffu, xhi = (i & 1) ? xp[i >> 1] & 0xffff0000u : xp[i >> 1] << 16;
#pragma unroll
      for (int k = 0; k < 4; ++k) {
        accA[2 * k] = dot2(wv[2 * i][k], xlo, accA[2 * k]);
        accA[2 * k + 1] = dot2(wv[2 * i][k], xhi, accA[2 * k + 1]);
        accB[2 * k] = dot2(wv[2 * i + 1][k], xlo, accB[2 * k]);
        accB[2 * k + 1] = dot2(wv[2 * i + 1][k], xhi, accB[2 * k + 1]);
      }
    }
    f32x2 ua = fold8(accA), ub = fold8(accB);
    ua.x += dpp<ROR8>(ua.x); ua.y += dpp<ROR8>(ua.y); ub.x += dpp<ROR8>(ub.x); ub.y += dpp<ROR8>(ub.y);
    const f32x2 u = c < 8 ? ua : ub;
    put2(rw, lane * 16, WS_QG + (h * NSPLIT + s) * 1024, u.x, u.y, tag32, tag4);
  } else {
    __syncthreads();
  }
  v4u gv, uv;
  {
    const unsigned gbytes = __builtin_amdgcn_readfirstlane((ffn && w >= 1 && w <= 10) ? 7 * F * 2 + 128 : 0);
    const size_t goff = (size_t)(80 * s + 8 * ((w + 15) & 15)) * F + 64 * (ffn ? h : 0);
    const bool gon = w >= 1 && w <= 10;
    const rsrc_t rg = mk(wg_ + (gon ? goff : 0), gbytes), ru = mk(wu_ + (gon ? goff : 0), gbytes);
    SB();
    gv = bld<NTA>(rg, rr * (F * 2) + 16 * (lane & 7), 0);
    uv = bld<NTA>(ru, rr * (F * 2) + 16 * (lane & 7), 0);
    SB();
  }
  v4u qp;
  unsigned tag32 = 0, tag4 = 0;
  float sc[8];
  unsigned pk[8];
  f32x4 acc = {0.f, 0.f, 0.f, 0.f};
  auto qk = [&](int i) INL {
    constexpr float SCALE = 0.08838834764831845f;
    float d = 0.f;
#pragma unroll
    for (int k = 0; k < 4; ++k) d = dot2(ld[i][k], qp[k], d);
    sc[i] = rbf(rbf(row_sum(d)) * SCALE);
  };
  auto pv = [&](int i) INL {
    acc = mfma_rows(pk[i], sel, ld[8 + i], acc);
    asm volatile("" : "+v"(acc));
  };
#pragma unroll
  for (int i = 0; i < WND; ++i) issue(i);
  auto step = [&](auto J) INL {
    constexpr int j = decltype(J)::value;
    if constexpr (j + WND < NLD - 1) {
      waitvm_not0<WND - 1>(w);
      issue(j + WND);
    }
    if constexpr (j == JX) if (w == 0) {
      const unsigned t32 = __builtin_amdgcn_readfirstlane(L.tag[0]), t4 = __builtin_amdgcn_readfirstlane(L.tag[1]);
      const f32x2 qs = gather_sum<8>(rw, lane * 16, WS_QG + h * NSPLIT * 1024, t32, t4);
      L.q[4 * c + r] = cvtpk(qs.x, qs.y);
      LDS_DRAIN();
      if (lane == 0) __hip_atomic_store((LAS unsigned*)&L.qflag, 1u, RLX_WG);
      SB();
    }
    if constexpr (j == JG) {
      asm volatile("" ::"v"(cpf));
      lds_wait(&L.xflag, 1u);
      tag32 = __builtin_amdgcn_readfirstlane(L.tag[0]); tag4 = __builtin_amdgcn_readfirstlane(L.tag[1]);
      if (ffn && w >= 1 && w <= 10) {
        const unsigned xw = L.xn[4 * (w - 1) + (rr >> 1)];
        const float xn = (rr & 1) ? bf_hi(xw) : bf_lo(xw);
        float g[8], u[8];
#pragma unroll
        for (int k = 0; k < 4; ++k) {
          g[2 * k] = xn * bf_lo(gv[k]); g[2 * k + 1] = xn * bf_hi(gv[k]);
          u[2 * k] = xn * bf_lo(uv[k]); u[2 * k + 1] = xn * bf_hi(uv[k]);
        }
        f32x2 gs = fold8(g), us = fold8(u);
        gs.x += dpp<ROR8>(gs.x); gs.y += dpp<ROR8>(gs.y); us.x += dpp<ROR8>(us.x); us.y += dpp<ROR8>(us.y);
        *(f32x2*)(L.gu + (w - 1) * 128 + 8 * c + 2 * r) = c < 8 ? gs : us;
        LDS_DRAIN();
        if (lane == 0) __hip_atomic_fetch_add((LAS unsigned*)&L.gcnt, 1u, RLX_WG);
      }
      SB();
    }
    if constexpr (j == JF) if (w == 0 && ffn) {
      lds_wait(&L.gcnt, 10u);
      float gg = 0.f, uu = 0.f;
#pragma unroll
      for (int t = 0; t < 10; ++t) { gg += L.gu[t * 128 + lane]; uu += L.gu[t * 128 + 64 + lane]; }
      put2(rw, lane * 16, WS_FG + (h * NSPLIT + s) * 1024, gg, uu, tag32, tag4);
      SB();
    }
    if constexpr (j == JQ) {
      lds_wait(&L.qflag, 1u);
      qp = *(const v4u*)(L.q + 4 * c);
    }
    if constexpr (j >= JQ && j < JQ + 4) {
      qk(2 * (j - JQ)); qk(2 * (j - JQ) + 1);
      asm volatile("" : "+v"(sc[2 * (j - JQ)]), "+v"(sc[2 * (j - JQ) + 1]));
      SB();
    }
    if constexpr (j == JQ + 4) {
      float m = sc[0];
#pragma unroll
      for (int i = 1; i < 8; ++i) m = fmaxf(m, sc[i]);
      const float m_w = xrow_max(m);
      float l = 0.f;
#pragma unroll
      for (int i = 0; i < 8; ++i) { const float pe = __builtin_amdgcn_exp2f((sc[i] - m_w) * LOG2E); l += pe; pk[i] = cvtpk(pe, pe); }
      const float l_w = xrow_sum(l);
      if (lane == 0) {
        *(f32x2*)(L.ml + 2 * w) = f32x2{m_w, l_w};
        LDS_DRAIN();
        __hip_atomic_fetch_add((LAS unsigned*)&L.mcnt, 1u, RLX_WG);
      }
      SB();
    }
    if constexpr (j == JQ + 5) {
#pragma unroll
      for (int i = 0; i <= j - 8; ++i) pv(i);
      SB();
    }
    if constexpr (j > JQ + 5) { pv(j - 8); SB(); }
  };
  step(IC<0>{});  step(IC<1>{});  step(IC<2>{});  step(IC<3>{});
  step(IC<4>{});  step(IC<5>{});  step(IC<6>{});  step(IC<7>{});
  step(IC<8>{});  step(IC<9>{});  step(IC<10>{});  step(IC<11>{});
  step(IC<12>{});  step(IC<13>{});  step(IC<14>{});  step(IC<15>{});
  if (lane < 32) *(f32x4*)(L.acc + w * 128 + 8 * c + 4 * r) = acc;
  if (w == 0) {
    wait_until(tstart + (unsigned)FG_TICK);
    float hv = 0.f;
    if (ffn) {
      const f32x2 gu = gather_sum<8>(rw, lane * 16, WS_FG + h * NSPLIT * 1024, tag32, tag4);
      const float g = rbf(gu.x), u = rbf(gu.y);
      const float sg = rbf(1.f / (1.f + __builtin_amdgcn_exp2f(-g * LOG2E)));
      hv = rbf(sg * u);
    }
    L.hv[lane] = cvtpk(hv, hv);
    lds_wait(&L.mcnt, (unsigned)NWAVE);
    const f32x2 ml = *(const f32x2*)(L.ml + 2 * c);
    const float m8 = max8(ml.x), M = fmaxf(m8, dpp<ROR8>(m8));
    const float fl = __builtin_amdgcn_exp2f((ml.x - M) * LOG2E);
    const float Ls = row_sum(fl * ml.y);
    put2(rw, lane == 0 ? 0u : OOB, WS_SG + h * 128 + s * 16, M, Ls, tag32, tag4);
    float fw[NWAVE];
#pragma unroll
    for (int t = 0; t < NWAVE; ++t) fw[t] = rdlane(fl, t);
    SB();
    __syncthreads();
    SB();
    f32x2 a[NWAVE];
#pragma unroll
    for (int t = 0; t < NWAVE; ++t) a[t] = *(const f32x2*)(L.acc + t * 128 + 2 * lane);
    f32x2 oa = a[0] * fw[0], ob = a[1] * fw[1];
#pragma unroll
    for (int t = 2; t < NWAVE; t += 2) { oa += a[t] * fw[t]; ob += a[t + 1] * fw[t + 1]; }
    put2(rw, lane * 16, WS_OG + (h * NSPLIT + s) * 1024, oa.x + ob.x, oa.y + ob.y, tag32, tag4);
    SB();
    issue(18);
    f32x2 o2 = {__uint_as_float(0x7fc00000u), __uint_as_float(0x7fc00000u)};
    __builtin_amdgcn_s_sleep(ODELAY);
#pragma nounroll
    for (unsigned spins = 0; spins < SPIN_CAP; ++spins) {
      asm volatile("" ::: "memory");
      const v4u xs = bld<SC1>(rw, (lane & 7) * 16, WS_SG + h * 128);
      v4u x[8];
#pragma unroll
      for (int k = 0; k < 8; ++k) x[k] = bld<SC1>(rw, lane * 16, WS_OG + (h * NSPLIT + k) * 1024);
      SB();
      TagChk tc;
      const f32x2 st = tc.take(xs, tag32, tag4);
      const float MM = max8(st.x);
      const float fs = __builtin_amdgcn_exp2f((st.x - MM) * LOG2E);
      const float ws8 = fs * __builtin_amdgcn_rcpf(sum8(fs * st.y));
      float wk[8];
#pragma unroll
      for (int k = 0; k < 8; ++k) wk[k] = rdlane(ws8, k);
      SB();
      f32x2 acc2 = tc.take(x[0], tag32, tag4) * wk[0];
#pragma unroll
      for (int k = 1; k < 8; ++k) acc2 += tc.take(x[k], tag32, tag4) * wk[k];
      asm volatile("" : "+v"(acc2));
      if (tc.ok()) { o2 = acc2; break; }
      __builtin_amdgcn_s_sleep(1);
    }
    *(v2u*)(L.o + 2 * lane) = v2u{cvtpk(o2.x, o2.x), cvtpk(o2.y, o2.y)};
  } else {
    __syncthreads();
    SB();
    issue(18);
  }
  f32x4 pa = mfma_rows(L.hv[rho], sel, ld[18], f32x4{0.f, 0.f, 0.f, 0.f});
  asm volatile("" : "+v"(pa));
  SB();
  __syncthreads();
  SB();
  {
    const v2u oc = *(const v2u*)(L.o + 2 * rho);
    pa = mfma_rows(oc.x, sel, ld[16], pa);
    const f32x4 pb = mfma_rows(oc.y, sel, ld[17], f32x4{0.f, 0.f, 0.f, 0.f});
    if (lane < 32) *(f32x4*)(L.p + w * 128 + 8 * c + 4 * r) = pa + pb;
  }
  __syncthreads();
  if (tid < 40) {
    f32x2 a[NWAVE];
#pragma unroll
    for (int t = 0; t < NWAVE; ++t) a[t] = *(const f32x2*)(L.p + t * 128 + 2 * tid);
#pragma unroll
    for (int st = 1; st < NWAVE; st *= 2)
#pragma unroll
      for (int t = 0; t < NWAVE; t += 2 * st) a[t] += a[t + st];
    put2(rw, (tid >> 3) * 4096 + (tid & 7) * 16, WS_PG + s * 5 * 4096 + h * 128, a[0].x, a[0].y, tag32, tag4);
  }
  if (w == 2 && h < 5) {
    const int col = 80 * s + 16 * h + 2 * (lane & 7);
    const unsigned xv = *(const GAS unsigned*)((const GAS unsigned short*)x_ + col);
    const unsigned long long enext = (((unsigned long long)L.tag[3] << 32) | L.tag[2]) + 1ull;
    __builtin_amdgcn_s_sleep(PDELAY);
    f32x2 a = gather_sum<4>(rw, lane * 16, WS_PG + (s * 5 + h) * 4096, tag32, tag4);
    a.x += dpp<ROR8>(a.x); a.y += dpp<ROR8>(a.y);
    a.x = fold16(a.x, a.y);
    a.x = fold32(a.x, a.x);
    const float xr = r == 0 ? bf_lo(xv) : bf_hi(xv);
    if (lane < 32 && c < 8) ((GAS unsigned short*)out_)[col + r] = (unsigned short)cvtpk(xr + a.x, 0.f);
    if (b == 0 && lane == 0) __hip_atomic_store(ws, enext, RLX_AGENT);
  }
  asm volatile(".Lkcode_end:" ::: "memory");
}

extern "C" void kernel_launch(void* const* d_in, const int* in_sizes, int n_in, void* d_out, int out_size, void* d_ws,
                              size_t ws_size, hipStream_t stream) {
  (void)in_sizes; (void)n_in; (void)out_size; (void)ws_size;
  hipLaunchKernelGGL(fused_decode, dim3(256), dim3(NT), 0, stream, (const unsigned short*)d_in[0], (const unsigned short*)d_in[1],
                     (const unsigned short*)d_in[2], (const unsigned short*)d_in[3], (const unsigned short*)d_in[4],
                     (const unsigned short*)d_in[6], (const unsigned short*)d_in[7], (const unsigned short*)d_in[8],
                     (unsigned short*)d_out, (unsigned long long*)d_ws);
}
```
